```python
import torch

def _softmax(x, axis=-1):
    return torch.softmax(x, dim=axis)
def _gelu(x):
    return torch.nn.functional.gelu(x, approximate="tanh")
def _silu(x):
    return torch.nn.functional.silu(x)
def _sigmoid(x):
    return torch.sigmoid(x)
def _layernorm(x, axis=-1, eps=1e-5):
    m = x.mean(dim=axis, keepdim=True)
    v = x.var(dim=axis, keepdim=True, unbiased=False)
    return (x - m) / torch.sqrt(v + eps)
def _scatter_add(tgt, idx, src, axis=0):
    return tgt.clone().index_add_(axis, idx, src)
def _segment_sum(src, seg, n):
    out = torch.zeros((n,) + src.shape[1:], dtype=src.dtype)
    return out.index_add_(0, seg, src)
def _pad(x, pw):
    flat = tuple(v for pair in reversed(pw) for v in pair)
    return torch.nn.functional.pad(x, flat)

kernel_name = 'standalone__w4a16_gemm_dequant__prompt'

def setup_inputs():

    _w = (lambda a: (lambda t: t.to(torch.bfloat16) if t.is_floating_point() else t)(torch.as_tensor(a)))
    B, S, D, F = 1, 2048, 576, 4608
    G = 32 if 32 > 0 else D
    D = (D // G) * G
    ng = D // G
    qw = torch.randint(0, 16, (D, F)).to(torch.int32)
    return {
        "x": _w((lambda *s: torch.randn(*s, dtype=torch.bfloat16) * 0.05)(B, S, D)),
        "qw": _w(qw),
        "scales": _w(torch.abs((lambda *s: torch.randn(*s, dtype=torch.bfloat16) * 0.05)(ng, F)) + 0.01),
        "zeros": _w(torch.full((ng, F), 8.0, dtype=float)),
    }

def reference(x, qw, scales, zeros):
    B, S, D = x.shape
    ng, F = scales.shape
    G = D // ng
    s = (lambda x, n, axis: torch.repeat_interleave(x, n, dim=axis))(scales, G, axis=0)
    z = (lambda x, n, axis: torch.repeat_interleave(x, n, dim=axis))(zeros, G, axis=0)
    w = ((lambda x, dt: x.to(dt))(qw, x.dtype) - z) * s
    return x @ w
```

```cpp
#include <hip/hip_runtime.h>
#include <hip/hip_bf16.h>
#include <stdint.h>

typedef __attribute__((ext_vector_type(8))) __bf16 bf16x8;
typedef __attribute__((ext_vector_type(4))) float f32x4;
typedef __attribute__((ext_vector_type(4))) uint32_t u32x4;

constexpr int M = 2048, K = 576, N = 4608, G = 32;

static __device__ __forceinline__ float bf2f(uint16_t v) { return __uint_as_float((uint32_t)v << 16); }

__global__ __launch_bounds__(256, 1) void k_base(const uint16_t* __restrict__ x, const int* __restrict__ qw,
                                                 const uint16_t* __restrict__ sc, const uint16_t* __restrict__ zr,
                                                 uint16_t* __restrict__ out) {
    const int b = blockIdx.x;
    const int panel = b % 36, ms = b / 36;
    const int lane = threadIdx.x & 63, wave = threadIdx.x >> 6;
    const int c = lane & 15, g = lane >> 4;
    const int n0 = panel * 128 + wave * 32;
    bf16x8 w[18][2];
#pragma unroll
    for (int s = 0; s < 18; ++s) {
#pragma unroll
        for (int t = 0; t < 2; ++t) {
            const int col = n0 + 8 * (c >> 2) + 4 * t + (c & 3);
            const float sv = bf2f(sc[s * N + col]), zv = bf2f(zr[s * N + col]);
#pragma unroll
            for (int j = 0; j < 8; ++j) {
                const int q = qw[(s * 32 + 8 * g + j) * N + col];
                const float d = (float)(__bf16)((float)q - zv);
                w[s][t][j] = (__bf16)(d * sv);
            }
        }
    }
    const int t0 = ms * 18 + (ms < 2 ? ms : 2), nt = 18 + (ms < 2 ? 1 : 0);
    for (int rt = t0; rt < t0 + nt; ++rt) {
        f32x4 a0 = {0, 0, 0, 0}, a1 = {0, 0, 0, 0};
        const uint16_t* xr = x + (rt * 16 + c) * K + 8 * g;
#pragma unroll
        for (int s = 0; s < 18; ++s) {
            bf16x8 xv = *(const bf16x8*)(xr + s * 32);
            a0 = __builtin_amdgcn_mfma_f32_16x16x32_bf16(w[s][0], xv, a0, 0, 0, 0);
            a1 = __builtin_amdgcn_mfma_f32_16x16x32_bf16(w[s][1], xv, a1, 0, 0, 0);
        }
        bf16x8 o;
#pragma unroll
        for (int r = 0; r < 4; ++r) { o[r] = (__bf16)a0[r]; o[4 + r] = (__bf16)a1[r]; }
        *(bf16x8*)(out + (size_t)(rt * 16 + c) * N + n0 + 8 * g) = o;
    }
}

extern "C" void kernel_launch(void* const* d_in, const int* in_sizes, int n_in, void* d_out, int out_size,
                              void* d_ws, size_t ws_size, hipStream_t stream) {
    k_base<<<252, 256, 0, stream>>>((const uint16_t*)d_in[0], (const int*)d_in[1], (const uint16_t*)d_in[2],
                                    (const uint16_t*)d_in[3], (uint16_t*)d_out);
}
```

```cpp
#include <hip/hip_runtime.h>
#include <stdint.h>

__global__ __launch_bounds__(256, 1) void w4a16_gemm(const uint16_t* __restrict__ x, const int* __restrict__ qw,
                                                     const uint16_t* __restrict__ sc, const uint16_t* __restrict__ zr,
                                                     uint16_t* __restrict__ out) {
    __shared__ __attribute__((aligned(16))) char lds[163840];
    const uint32_t b = blockIdx.x;
    const uint32_t xcd = b & 7, idx = b >> 3;
    const uint32_t L = (xcd & 3) * 63 + (xcd >> 2) * 32 + idx;
    const uint32_t panel = L / 7, ms = L - panel * 7;
    const uint32_t w = __builtin_amdgcn_readfirstlane(threadIdx.x >> 6);
    const uint32_t kh = w >> 1;
    const uint32_t t0 = ms * 18 + (ms < 2 ? ms : 2), nt = 18 + (ms < 2 ? 1 : 0);
    const uint32_t krot = ((ms * 5) % 7 * 9 + 3) / 7;
    const uint32_t trot = ((xcd & 3) * nt) >> 2;
    const uint32_t nh = panel * 128 + w * 32;
    const uint32_t wc = nh * 4;
    const uint32_t ta = (t0 + trot) * 18432, tk = ta + kh * 18432, lim = (t0 + nt) * 18432, span = nt * 18432;
    const uint32_t sc0 = nh * 2, oc = nh * 2;
    uint32_t tid = threadIdx.x;
    asm volatile(
        "s_mov_b64 s[28:29], %[pq]\n"
        "s_mov_b32 s30, 10616832\n"
        "s_mov_b32 s31, 0x20000\n"
        "v_and_b32 v1, 7, %[tid]\n"
        "v_bfe_u32 v2, %[tid], 4, 2\n"
        "v_lshlrev_b32 v1, 4, v1\n"
        "v_lshrrev_b32 v3, 1, %[tid]\n"
        "v_and_or_b32 v2, v3, 4, v2\n"
        "v_mul_u32_u24 v2, 0x24000, v2\n"
        "s_mov_b32 s44, %[x0]\n"
        "v_add3_u32 v1, v1, v2, %[wc]\n"
        "v_add_u32 v2, 18432, v1\n"
        "v_add_u32 v3, 36864, v1\n"
        "v_add_u32 v4, 55296, v1\n"
        "s_mul_i32 s33, s44, 9216\n"
        "s_add_u32 s34, s33, 73728\n"
        "buffer_load_dwordx4 v[160:163], v1, s[28:31], s33 offen\n"
        "s_add_u32 s45, s44, 128\n"
        "s_cmp_eq_u32 s45, 1152\n"
        "s_cselect_b32 s45, 0, s45\n"
        "s_add_u32 s46, s45, 128\n"
        "s_cmp_eq_u32 s46, 1152\n"
        "s_cselect_b32 s46, 0, s46\n"
        "buffer_load_dwordx4 v[164:167], v2, s[28:31], s33 offen\n"
        "s_add_u32 s47, s46, 128\n"
        "s_cmp_eq_u32 s47, 1152\n"
        "s_cselect_b32 s47, 0, s47\n"
        "s_add_u32 s48, s47, 128\n"
        "s_cmp_eq_u32 s48, 1152\n"
        "s_cselect_b32 s48, 0, s48\n"
        "s_add_u32 s49, s48, 128\n"
        "s_cmp_eq_u32 s49, 1152\n"
        "s_cselect_b32 s49, 0, s49\n"
        "s_add_u32 s50, s49, 128\n"
        "s_cmp_eq_u32 s50, 1152\n"
        "s_cselect_b32 s50, 0, s50\n"
        "buffer_load_dwordx4 v[168:171], v3, s[28:31], s33 offen\n"
        "s_add_u32 s51, s50, 128\n"
        "s_cmp_eq_u32 s51, 1152\n"
        "s_cselect_b32 s51, 0, s51\n"
        "s_add_u32 s52, s51, 128\n"
        "s_cmp_eq_u32 s52, 1152\n"
        "s_cselect_b32 s52, 0, s52\n"
        "v_bfe_u32 v8, %[tid], 4, 2\n"
        "v_and_b32 v10, 7, %[tid]\n"
        "buffer_load_dwordx4 v[172:175], v4, s[28:31], s33 offen\n"
        "v_add_u32 v8, %[krot], v8\n"
        "v_lshl_add_u32 v10, v10, 3, %[sc0]\n"
        "v_add_u32 v9, 4, v8\n"
        "v_bfe_u32 v11, %[tid], 3, 1\n"
        "buffer_load_dwordx4 v[176:179], v1, s[28:31], s34 offen\n"
        "v_subrev_u32 v12, 9, v8\n"
        "v_subrev_u32 v13, 9, v9\n"
        "v_mul_u32_u24 v11, 9216, v11\n"
        "v_min_u32 v8, v8, v12\n"
        "buffer_load_dwordx4 v[180:183], v2, s[28:31], s34 offen\n"
        "v_min_u32 v9, v9, v13\n"
        "v_add_u32 v10, v10, v11\n"
        "v_mul_u32_u24 v8, 18432, v8\n"
        "v_mul_u32_u24 v9, 18432, v9\n"
        "buffer_load_dwordx4 v[184:187], v3, s[28:31], s34 offen\n"
        "s_mov_b64 s[40:41], %[ps]\n"
        "s_mov_b32 s42, 165888\n"
        "s_mov_b32 s43, 0x20000\n"
        "v_add_u32 v8, v8, v10\n"
        "buffer_load_dwordx4 v[188:191], v4, s[28:31], s34 offen\n"
        "v_add_u32 v9, v9, v10\n"
        "s_mul_i32 s35, s52, 144\n"
        "buffer_load_dwordx2 v[112:113], v8, s[40:43], 0 offen\n"
        "buffer_load_dwordx2 v[116:117], v9, s[40:43], 0 offen\n"
        "buffer_load_dwordx2 v[120:121], v10, s[40:43], s35 offen\n"
        "s_mov_b64 s[40:41], %[pz]\n"
        "buffer_load_dwordx2 v[114:115], v8, s[40:43], 0 offen\n"
        "buffer_load_dwordx2 v[118:119], v9, s[40:43], 0 offen\n"
        "buffer_load_dwordx2 v[122:123], v10, s[40:43], s35 offen\n"
        "v_bfe_u32 v5, %[tid], 3, 3\n"
        "v_lshrrev_b32 v6, 1, v5\n"
        "s_and_b32 s54, %[w], 1\n"
        "s_lshl_b32 s53, s54, 2\n"
        "v_or_b32 v6, s53, v6\n"
        "v_and_b32 v7, 7, %[tid]\n"
        "v_xor_b32 v6, v6, v7\n"
        "v_mul_u32_u24 v5, 1152, v5\n"
        "v_lshl_add_u32 v5, v6, 4, v5\n"
        "s_mul_i32 s53, s54, 9216\n"
        "v_add_u32 v5, s53, v5\n"
        "s_lshl_b32 s54, s54, 10\n"
        "s_mov_b32 s98, s54\n"
        "s_lshr_b32 s53, %[w], 1\n"
        "s_mul_i32 s55, s53, 18432\n"
        "s_add_u32 s55, s55, s54\n"
        "s_lshl_b32 s53, s53, 11\n"
        "s_add_u32 s54, s54, s53\n"
        "s_mov_b64 s[36:37], %[px]\n"
        "s_mov_b32 s38, 2359296\n"
        "s_mov_b32 s39, 0x20000\n"
        "v_add_u32 v5, s44, v5\n"
        "s_add_u32 s59, %[tk], 0\n"
        "s_cmp_ge_u32 s59, %[lim]\n"
        "s_cselect_b32 s53, %[span], 0\n"
        "s_sub_u32 s59, s59, s53\n"
        "s_add_u32 s60, %[tk], 36864\n"
        "s_cmp_ge_u32 s60, %[lim]\n"
        "s_cselect_b32 s53, %[span], 0\n"
        "s_sub_u32 s60, s60, s53\n"
        "s_add_u32 s61, %[tk], 73728\n"
        "s_cmp_ge_u32 s61, %[lim]\n"
        "s_cselect_b32 s53, %[span], 0\n"
        "s_sub_u32 s61, s61, s53\n"
        "s_add_u32 s62, %[tk], 110592\n"
        "s_cmp_ge_u32 s62, %[lim]\n"
        "s_cselect_b32 s53, %[span], 0\n"
        "s_sub_u32 s62, s62, s53\n"
        "s_add_u32 s63, %[tk], 147456\n"
        "s_cmp_ge_u32 s63, %[lim]\n"
        "s_cselect_b32 s53, %[span], 0\n"
        "s_sub_u32 s63, s63, s53\n"
        "s_add_u32 s64, %[tk], 184320\n"
        "s_cmp_ge_u32 s64, %[lim]\n"
        "s_cselect_b32 s53, %[span], 0\n"
        "s_sub_u32 s64, s64, s53\n"
        "s_add_u32 m0, s54, 0\n"
        "s_nop 0\n"
        "buffer_load_dwordx4 v5, s[36:39], s59 offen  lds\n"
        "s_add_u32 m0, s54, 4096\n"
        "s_nop 0\n"
        "buffer_load_dwordx4 v5, s[36:39], s60 offen  lds\n"
        "s_add_u32 m0, s54, 8192\n"
        "s_nop 0\n"
        "buffer_load_dwordx4 v5, s[36:39], s61 offen  lds\n"
        "s_add_u32 m0, s54, 12288\n"
        "s_nop 0\n"
        "buffer_load_dwordx4 v5, s[36:39], s62 offen  lds\n"
        "s_add_u32 m0, s54, 16384\n"
        "s_nop 0\n"
        "buffer_load_dwordx4 v5, s[36:39], s63 offen  lds\n"
        "s_add_u32 m0, s54, 20480\n"
        "s_nop 0\n"
        "buffer_load_dwordx4 v5, s[36:39], s64 offen  lds\n"
        "s_getpc_b64 s[56:57]\n"
        ".Lpc_%=:\n"
        "v_lshlrev_b32 v10, 7, %[tid]\n"
        "v_min_u32 v10, .Lend_%=-.Lpc_%=-4, v10\n"
        "global_load_dword v159, v10, s[56:57]\n"
        "s_mul_i32 s58, %[w], 4608\n"
        "s_add_u32 s58, s58, 145408\n"
        "v_and_b32 v7, 15, %[tid]\n"
        "v_lshl_add_u32 v6, v7, 4, s58\n"
        "s_mul_i32 s33, s45, 9216\n"
        "s_mov_b32 vcc_lo, 0xff00ff\n"
        "s_mov_b32 vcc_hi, 0xff00ff\n"
        "v_bfe_u32 v8, %[tid], 4, 2\n"
        "v_lshl_add_u32 v11, v8, 9, v6\n"
        "v_lshrrev_b32 v9, 1, v7\n"
        "v_xor_b32 v8, v8, v9\n"
        "v_lshlrev_b32 v7, 7, v7\n"
        "s_add_u32 s34, s33, 73728\n"
        "v_lshl_add_u32 v7, v8, 4, v7\n"
        "v_xor_b32 v0, 64, v7\n"
        "s_mov_b64 s[40:41], %[po]\n"
        "s_mov_b32 s42, 18874368\n"
        "s_waitcnt vmcnt(12)\n"
        "v_lshlrev_b32 v128, 16, v112\n"
        "v_and_b32 v132, 0xffff0000, v112\n"
        "v_lshlrev_b32 v129, 16, v113\n"
        "buffer_load_dwordx4 v[192:195], v1, s[28:31], s33 offen\n"
        "v_and_b32 v133, 0xffff0000, v113\n"
        "s_waitcnt vmcnt(10)\n"
        "v_lshlrev_b32 v130, 16, v114\n"
        "v_and_b32 v134, 0xffff0000, v114\n"
        "v_lshlrev_b32 v131, 16, v115\n"
        "v_and_b32 v135, 0xffff0000, v115\n"
        "v_mul_f32 v130, -v130, v128\n"
        "v_mul_f32 v131, -v131, v129\n"
        "buffer_load_dwordx4 v[196:199], v2, s[28:31], s33 offen\n"
        "v_mul_f32 v128, 0x4b800000, v128\n"
        "v_mul_f32 v129, 0x4b800000, v129\n"
        "ds_write_b128 v11, v[128:131] offset:0\n"
        "v_mul_f32 v134, -v134, v132\n"
        "v_mul_f32 v135, -v135, v133\n"
        "v_mul_f32 v132, 0x4b800000, v132\n"
        "v_mul_f32 v133, 0x4b800000, v133\n"
        "buffer_load_dwordx4 v[200:203], v3, s[28:31], s33 offen\n"
        "ds_write_b128 v11, v[132:135] offset:256\n"
        "v_lshlrev_b32 v136, 16, v116\n"
        "v_and_b32 v140, 0xffff0000, v116\n"
        "v_lshlrev_b32 v137, 16, v117\n"
        "v_and_b32 v141, 0xffff0000, v117\n"
        "s_waitcnt vmcnt(11)\n"
        "v_lshlrev_b32 v138, 16, v118\n"
        "v_and_b32 v142, 0xffff0000, v118\n"
        "buffer_load_dwordx4 v[204:207], v4, s[28:31], s33 offen\n"
        "v_lshlrev_b32 v139, 16, v119\n"
        "v_and_b32 v143, 0xffff0000, v119\n"
        "v_mul_f32 v138, -v138, v136\n"
        "v_mul_f32 v139, -v139, v137\n"
        "v_mul_f32 v136, 0x4b800000, v136\n"
        "v_mul_f32 v137, 0x4b800000, v137\n"
        "ds_write_b128 v11, v[136:139] offset:2048\n"
        "buffer_load_dwordx4 v[208:211], v1, s[28:31], s34 offen\n"
        "v_mul_f32 v142, -v142, v140\n"
        "v_mul_f32 v143, -v143, v141\n"
        "v_mul_f32 v140, 0x4b800000, v140\n"
        "v_mul_f32 v141, 0x4b800000, v141\n"
        "ds_write_b128 v11, v[140:143] offset:2304\n"
        "v_lshlrev_b32 v128, 16, v120\n"
        "v_and_b32 v132, 0xffff0000, v120\n"
        "buffer_load_dwordx4 v[212:215], v2, s[28:31], s34 offen\n"
        "v_lshlrev_b32 v129, 16, v121\n"
        "v_and_b32 v133, 0xffff0000, v121\n"
        "s_waitcnt vmcnt(13)\n"
        "v_lshlrev_b32 v130, 16, v122\n"
        "v_and_b32 v134, 0xffff0000, v122\n"
        "v_lshlrev_b32 v131, 16, v123\n"
        "v_and_b32 v135, 0xffff0000, v123\n"
        "v_mul_f32 v130, -v130, v128\n"
        "buffer_load_dwordx4 v[216:219], v3, s[28:31], s34 offen\n"
        "v_mul_f32 v131, -v131, v129\n"
        "v_mul_f32 v128, 0x4b800000, v128\n"
        "v_mul_f32 v129, 0x4b800000, v129\n"
        "ds_write_b128 v6, v[128:131] offset:4096\n"
        "v_mul_f32 v134, -v134, v132\n"
        "v_mul_f32 v135, -v135, v133\n"
        "v_mul_f32 v132, 0x4b800000, v132\n"
        "buffer_load_dwordx4 v[220:223], v4, s[28:31], s34 offen\n"
        "v_mul_f32 v133, 0x4b800000, v133\n"
        "ds_write_b128 v6, v[132:135] offset:4352\n"
        "ds_read_b128 v[8:11], v6 offset:0\n"
        "ds_read_b128 v[12:15], v6 offset:256\n"
        "s_waitcnt lgkmcnt(1)\n"
        "v_fma_mix_f32 v160, v160, v8, v10 op_sel_hi:[1,0,0]\n"
        "v_fma_mix_f32 v164, v164, v8, v10 op_sel_hi:[1,0,0]\n"
        "v_fma_mix_f32 v162, v162, v9, v11 op_sel_hi:[1,0,0]\n"
        "v_fma_mix_f32 v166, v166, v9, v11 op_sel_hi:[1,0,0]\n"
        "v_fma_mix_f32 v168, v168, v8, v10 op_sel_hi:[1,0,0]\n"
        "v_fma_mix_f32 v172, v172, v8, v10 op_sel_hi:[1,0,0]\n"
        "v_fma_mix_f32 v170, v170, v9, v11 op_sel_hi:[1,0,0]\n"
        "v_fma_mix_f32 v174, v174, v9, v11 op_sel_hi:[1,0,0]\n"
        "v_cvt_pk_bf16_f32 v160, v160, v164\n"
        "v_cvt_pk_bf16_f32 v24, v162, v166\n"
        "v_fma_mix_f32 v176, v176, v8, v10 op_sel_hi:[1,0,0]\n"
        "v_fma_mix_f32 v180, v180, v8, v10 op_sel_hi:[1,0,0]\n"
        "v_fma_mix_f32 v178, v178, v9, v11 op_sel_hi:[1,0,0]\n"
        "v_fma_mix_f32 v182, v182, v9, v11 op_sel_hi:[1,0,0]\n"
        "v_cvt_pk_bf16_f32 v168, v168, v172\n"
        "v_cvt_pk_bf16_f32 v25, v170, v174\n"
        "v_cndmask_b32_dpp v16, v24, v160, vcc row_ror:8 row_mask:0xf bank_mask:0xf\n"
        "v_mov_b32_dpp v24, v160 row_ror:8 row_mask:0xf bank_mask:0x3\n"
        "v_fma_mix_f32 v184, v184, v8, v10 op_sel_hi:[1,0,0]\n"
        "v_fma_mix_f32 v188, v188, v8, v10 op_sel_hi:[1,0,0]\n"
        "v_fma_mix_f32 v186, v186, v9, v11 op_sel_hi:[1,0,0]\n"
        "v_fma_mix_f32 v190, v190, v9, v11 op_sel_hi:[1,0,0]\n"
        "v_cvt_pk_bf16_f32 v176, v176, v180\n"
        "v_cvt_pk_bf16_f32 v26, v178, v182\n"
        "v_cndmask_b32_dpp v17, v25, v168, vcc row_ror:8 row_mask:0xf bank_mask:0xf\n"
        "v_mov_b32_dpp v25, v168 row_ror:8 row_mask:0xf bank_mask:0x3\n"
        "v_cvt_pk_bf16_f32 v184, v184, v188\n"
        "v_cvt_pk_bf16_f32 v27, v186, v190\n"
        "v_cndmask_b32_dpp v18, v26, v176, vcc row_ror:8 row_mask:0xf bank_mask:0xf\n"
        "v_mov_b32_dpp v26, v176 row_ror:8 row_mask:0xf bank_mask:0x3\n"
        "v_cndmask_b32_dpp v19, v27, v184, vcc row_ror:8 row_mask:0xf bank_mask:0xf\n"
        "v_mov_b32_dpp v27, v184 row_ror:8 row_mask:0xf bank_mask:0x3\n"
        "s_waitcnt vmcnt(9)\n"
        "s_barrier\n"
        "ds_read_b128 a[96:99], v7 offset:0\n"
        "ds_read_b128 a[100:103], v7 offset:2048\n"
        "ds_read_b128 a[104:107], v7 offset:4096\n"
        "ds_read_b128 a[108:111], v7 offset:6144\n"
        "ds_read_b128 a[112:115], v7 offset:8192\n"
        "ds_read_b128 a[116:119], v7 offset:10240\n"
        "ds_read_b128 a[120:123], v7 offset:12288\n"
        "ds_read_b128 a[124:127], v7 offset:14336\n"
        "ds_read_b128 a[128:131], v7 offset:16384\n"
        "ds_read_b128 a[132:135], v7 offset:18432\n"
        "ds_read_b128 a[136:139], v7 offset:20480\n"
        "ds_read_b128 a[140:143], v7 offset:22528\n"
        "s_waitcnt lgkmcnt(6)\n"
        "v_mfma_f32_16x16x32_bf16 a[0:3], a[96:99], v[16:19], 0\n"
        "ds_read_b128 a[144:147], v0 offset:0\n"
        "s_sub_u32 s35, s45, s44\n"
        "ds_read_b128 v[8:11], v6 offset:512\n"
        "v_fma_mix_f32 v161, v161, v12, v14 op_sel_hi:[1,0,0]\n"
        "v_mfma_f32_16x16x32_bf16 a[8:11], a[100:103], v[16:19], 0\n"
        "s_add_u32 s65, %[tk], 221184\n"
        "s_cmp_ge_u32 s65, %[lim]\n"
        "s_cselect_b32 s53, %[span], 0\n"
        "s_sub_u32 s65, s65, s53\n"
        "ds_read_b128 a[148:151], v0 offset:2048\n"
        "v_add_u32 v5, s35, v5\n"
        "v_fma_mix_f32 v165, v165, v12, v14 op_sel_hi:[1,0,0]\n"
        "v_mfma_f32_16x16x32_bf16 a[16:19], a[104:107], v[16:19], 0\n"
        "s_add_u32 m0, s54, 24576\n"
        "ds_read_b128 a[152:155], v0 offset:4096\n"
        "v_fma_mix_f32 v163, v163, v13, v15 op_sel_hi:[1,0,0]\n"
        "v_mfma_f32_16x16x32_bf16 a[24:27], a[108:111], v[16:19], 0\n"
        "s_add_u32 s66, %[tk], 258048\n"
        "s_cmp_ge_u32 s66, %[lim]\n"
        "s_cselect_b32 s53, %[span], 0\n"
        "s_sub_u32 s66, s66, s53\n"
        "ds_read_b128 a[156:159], v0 offset:6144\n"
        "buffer_load_dwordx4 v5, s[36:39], s59 offen  lds\n"
        "v_mfma_f32_16x16x32_bf16 a[32:35], a[112:115], v[16:19], 0\n"
        "v_fma_mix_f32 v167, v167, v13, v15 op_sel_hi:[1,0,0]\n"
        "s_add_u32 s67, %[tk], 294912\n"
        "s_cmp_ge_u32 s67, %[lim]\n"
        "s_cselect_b32 s53, %[span], 0\n"
        "s_sub_u32 s67, s67, s53\n"
        "ds_read_b128 a[160:163], v0 offset:8192\n"
        "s_add_u32 m0, s54, 28672\n"
        "v_mfma_f32_16x16x32_bf16 a[40:43], a[116:119], v[16:19], 0\n"
        "v_fma_mix_f32 v169, v169, v12, v14 op_sel_hi:[1,0,0]\n"
        "buffer_load_dwordx4 v5, s[36:39], s60 offen  lds\n"
        "ds_read_b128 a[164:167], v0 offset:10240\n"
        "s_waitcnt lgkmcnt(7)\n"
        "v_mfma_f32_16x16x32_bf16 a[48:51], a[120:123], v[16:19], 0\n"
        "ds_read_b128 a[168:171], v0 offset:12288\n"
        "s_add_u32 s68, %[ta], 331776\n"
        "s_cmp_ge_u32 s68, %[lim]\n"
        "s_cselect_b32 s53, %[span], 0\n"
        "s_sub_u32 s68, s68, s53\n"
        "v_fma_mix_f32 v173, v173, v12, v14 op_sel_hi:[1,0,0]\n"
        "v_mfma_f32_16x16x32_bf16 a[56:59], a[124:127], v[16:19], 0\n"
        "s_add_u32 m0, s54, 32768\n"
        "ds_read_b128 a[172:175], v0 offset:14336\n"
        "buffer_load_dwordx4 v5, s[36:39], s61 offen  lds\n"
        "v_fma_mix_f32 v171, v171, v13, v15 op_sel_hi:[1,0,0]\n"
        "v_mfma_f32_16x16x32_bf16 a[64:67], a[128:131], v[16:19], 0\n"
        "s_add_u32 s69, %[ta], 0\n"
        "s_cmp_ge_u32 s69, %[lim]\n"
        "s_cselect_b32 s53, %[span], 0\n"
        "s_sub_u32 s69, s69, s53\n"
        "s_lshl_b32 s69, s69, 3\n"
        "ds_read_b128 a[176:179], v0 offset:16384\n"
        "v_fma_mix_f32 v175, v175, v13, v15 op_sel_hi:[1,0,0]\n"
        "v_mfma_f32_16x16x32_bf16 a[72:75], a[132:135], v[16:19], 0\n"
        "s_add_u32 m0, s54, 36864\n"
        "ds_read_b128 a[180:183], v0 offset:18432\n"
        "s_lshl_b32 s56, %[lim], 3\n"
        "s_sub_u32 s57, %[lim], %[span]\n"
        "s_lshl_b32 s57, s57, 3\n"
        "v_mfma_f32_16x16x32_bf16 a[80:83], a[136:139], v[16:19], 0\n"
        "v_cvt_pk_bf16_f32 v161, v161, v165\n"
        "buffer_load_dwordx4 v5, s[36:39], s62 offen  lds\n"
        "ds_read_b128 a[184:187], v0 offset:20480\n"
        "s_add_u32 m0, s54, 40960\n"
        "v_mfma_f32_16x16x32_bf16 a[88:91], a[140:143], v[16:19], 0\n"
        "v_cvt_pk_bf16_f32 v28, v163, v167\n"
        "s_add_u32 s70, s69, 147456\n"
        "s_cmp_eq_u32 s70, s56\n"
        "s_cselect_b32 s70, s57, s70\n"
        "ds_read_b128 a[188:191], v0 offset:22528\n"
        "s_waitcnt lgkmcnt(6)\n"
        "v_mfma_f32_16x16x32_bf16 a[0:3], a[144:147], v[24:27], a[0:3]\n"
        "v_fma_mix_f32 v177, v177, v12, v14 op_sel_hi:[1,0,0]\n"
        "buffer_load_dwordx4 v5, s[36:39], s63 offen  lds\n"
        "v_fma_mix_f32 v181, v181, v12, v14 op_sel_hi:[1,0,0]\n"
        "v_mfma_f32_16x16x32_bf16 a[8:11], a[148:151], v[24:27], a[8:11]\n"
        "s_add_u32 m0, s54, 45056\n"
        "v_fma_mix_f32 v179, v179, v13, v15 op_sel_hi:[1,0,0]\n"
        "s_add_u32 s71, s70, 147456\n"
        "s_cmp_eq_u32 s71, s56\n"
        "s_cselect_b32 s71, s57, s71\n"
        "v_fma_mix_f32 v183, v183, v13, v15 op_sel_hi:[1,0,0]\n"
        "v_mfma_f32_16x16x32_bf16 a[16:19], a[152:155], v[24:27], a[16:19]\n"
        "buffer_load_dwordx4 v5, s[36:39], s64 offen  lds\n"
        "v_cvt_pk_bf16_f32 v169, v169, v173\n"
        "s_mul_i32 s33, s46, 9216\n"
        "v_mfma_f32_16x16x32_bf16 a[24:27], a[156:159], v[24:27], a[24:27]\n"
        "v_cvt_pk_bf16_f32 v29, v171, v175\n"
        "s_add_u32 s72, s71, 147456\n"
        "s_cmp_eq_u32 s72, s56\n"
        "s_cselect_b32 s72, s57, s72\n"
        "v_cndmask_b32_dpp v20, v28, v161, vcc row_ror:8 row_mask:0xf bank_mask:0xf\n"
        "s_add_u32 s34, s33, 73728\n"
        "v_mfma_f32_16x16x32_bf16 a[32:35], a[160:163], v[24:27], a[32:35]\n"
        "v_mov_b32_dpp v28, v161 row_ror:8 row_mask:0xf bank_mask:0x3\n"
        "s_add_u32 s73, s72, 147456\n"
        "s_cmp_eq_u32 s73, s56\n"
        "s_cselect_b32 s73, s57, s73\n"
        "v_fma_mix_f32 v185, v185, v12, v14 op_sel_hi:[1,0,0]\n"
        "v_mfma_f32_16x16x32_bf16 a[40:43], a[164:167], v[24:27], a[40:43]\n"
        "buffer_load_dwordx4 v[224:227], v1, s[28:31], s33 offen\n"
        "v_fma_mix_f32 v189, v189, v12, v14 op_sel_hi:[1,0,0]\n"
        "buffer_load_dwordx4 v[228:231], v2, s[28:31], s33 offen\n"
        "v_fma_mix_f32 v187, v187, v13, v15 op_sel_hi:[1,0,0]\n"
        "s_waitcnt lgkmcnt(0)\n"
        "v_mfma_f32_16x16x32_bf16 a[48:51], a[168:171], v[24:27], a[48:51]\n"
        "v_fma_mix_f32 v191, v191, v13, v15 op_sel_hi:[1,0,0]\n"
        "s_add_u32 s74, s73, 147456\n"
        "s_cmp_eq_u32 s74, s56\n"
        "s_cselect_b32 s74, s57, s74\n"
        "v_cvt_pk_bf16_f32 v177, v177, v181\n"
        "v_mfma_f32_16x16x32_bf16 a[56:59], a[172:175], v[24:27], a[56:59]\n"
        "buffer_load_dwordx4 v[232:235], v3, s[28:31], s33 offen\n"
        "v_cvt_pk_bf16_f32 v30, v179, v183\n"
        "buffer_load_dwordx4 v[236:239], v4, s[28:31], s33 offen\n"
        "v_cndmask_b32_dpp v21, v29, v169, vcc row_ror:8 row_mask:0xf bank_mask:0xf\n"
        "v_mfma_f32_16x16x32_bf16 a[64:67], a[176:179], v[24:27], a[64:67]\n"
        "s_add_u32 s75, s74, 147456\n"
        "s_cmp_eq_u32 s75, s56\n"
        "s_cselect_b32 s75, s57, s75\n"
        "v_mov_b32_dpp v29, v169 row_ror:8 row_mask:0xf bank_mask:0x3\n"
        "buffer_load_dwordx4 v[240:243], v1, s[28:31], s34 offen\n"
        "v_mfma_f32_16x16x32_bf16 a[72:75], a[180:183], v[24:27], a[72:75]\n"
        "v_cvt_pk_bf16_f32 v185, v185, v189\n"
        "s_add_u32 s76, s75, 147456\n"
        "s_cmp_eq_u32 s76, s56\n"
        "s_cselect_b32 s76, s57, s76\n"
        "v_cvt_pk_bf16_f32 v31, v187, v191\n"
        "buffer_load_dwordx4 v[244:247], v2, s[28:31], s34 offen\n"
        "v_mfma_f32_16x16x32_bf16 a[80:83], a[184:187], v[24:27], a[80:83]\n"
        "v_cndmask_b32_dpp v22, v30, v177, vcc row_ror:8 row_mask:0xf bank_mask:0xf\n"
        "buffer_load_dwordx4 v[248:251], v3, s[28:31], s34 offen\n"
        "v_mov_b32_dpp v30, v177 row_ror:8 row_mask:0xf bank_mask:0x3\n"
        "v_mfma_f32_16x16x32_bf16 a[88:91], a[188:191], v[24:27], a[88:91]\n"
        "s_add_u32 s77, s76, 147456\n"
        "s_cmp_eq_u32 s77, s56\n"
        "s_cselect_b32 s77, s57, s77\n"
        "v_cndmask_b32_dpp v23, v31, v185, vcc row_ror:8 row_mask:0xf bank_mask:0xf\n"
        "buffer_load_dwordx4 v[252:255], v4, s[28:31], s34 offen\n"
        "v_mov_b32_dpp v31, v185 row_ror:8 row_mask:0xf bank_mask:0x3\n"
        "v_mfma_f32_16x16x32_bf16 a[4:7], a[96:99], v[20:23], 0\n"
        "ds_read_b128 v[12:15], v6 offset:768\n"
        "s_sub_u32 s35, s46, s45\n"
        "s_waitcnt vmcnt(18)\n"
        "v_fma_mix_f32 v192, v192, v8, v10 op_sel_hi:[1,0,0]\n"
        "v_mfma_f32_16x16x32_bf16 a[12:15], a[100:103], v[20:23], 0\n"
        "s_add_u32 s78, s77, 147456\n"
        "s_cmp_eq_u32 s78, s56\n"
        "s_cselect_b32 s78, s57, s78\n"
        "v_fma_mix_f32 v196, v196, v8, v10 op_sel_hi:[1,0,0]\n"
        "v_fma_mix_f32 v194, v194, v9, v11 op_sel_hi:[1,0,0]\n"
        "v_mfma_f32_16x16x32_bf16 a[20:23], a[104:107], v[20:23], 0\n"
        "v_add_u32 v5, s35, v5\n"
        "v_fma_mix_f32 v198, v198, v9, v11 op_sel_hi:[1,0,0]\n"
        "v_fma_mix_f32 v200, v200, v8, v10 op_sel_hi:[1,0,0]\n"
        "v_mfma_f32_16x16x32_bf16 a[28:31], a[108:111], v[20:23], 0\n"
        "s_add_u32 s79, s78, 147456\n"
        "s_cmp_eq_u32 s79, s56\n"
        "s_cselect_b32 s79, s57, s79\n"
        "v_fma_mix_f32 v204, v204, v8, v10 op_sel_hi:[1,0,0]\n"
        "s_add_u32 m0, s54, 49152\n"
        "v_mfma_f32_16x16x32_bf16 a[36:39], a[112:115], v[20:23], 0\n"
        "v_fma_mix_f32 v202, v202, v9, v11 op_sel_hi:[1,0,0]\n"
        "v_fma_mix_f32 v206, v206, v9, v11 op_sel_hi:[1,0,0]\n"
        "buffer_load_dwordx4 v5, s[36:39], s59 offen  lds\n"
        "v_mfma_f32_16x16x32_bf16 a[44:47], a[116:119], v[20:23], 0\n"
        "v_cvt_pk_bf16_f32 v192, v192, v196\n"
        "s_add_u32 s80, s79, 147456\n"
        "s_cmp_eq_u32 s80, s56\n"
        "s_cselect_b32 s80, s57, s80\n"
        "v_cvt_pk_bf16_f32 v40, v194, v198\n"
        "v_mfma_f32_16x16x32_bf16 a[52:55], a[120:123], v[20:23], 0\n"
        "s_waitcnt vmcnt(15)\n"
        "v_fma_mix_f32 v208, v208, v8, v10 op_sel_hi:[1,0,0]\n"
        "s_add_u32 m0, s54, 53248\n"
        "v_fma_mix_f32 v212, v212, v8, v10 op_sel_hi:[1,0,0]\n"
        "v_mfma_f32_16x16x32_bf16 a[60:63], a[124:127], v[20:23], 0\n"
        "s_add_u32 s81, s80, 147456\n"
        "s_cmp_eq_u32 s81, s56\n"
        "s_cselect_b32 s81, s57, s81\n"
        "v_fma_mix_f32 v210, v210, v9, v11 op_sel_hi:[1,0,0]\n"
        "v_fma_mix_f32 v214, v214, v9, v11 op_sel_hi:[1,0,0]\n"
        "v_mfma_f32_16x16x32_bf16 a[68:71], a[128:131], v[20:23], 0\n"
        "buffer_load_dwordx4 v5, s[36:39], s60 offen  lds\n"
        "v_cvt_pk_bf16_f32 v200, v200, v204\n"
        "v_cvt_pk_bf16_f32 v41, v202, v206\n"
        "v_mfma_f32_16x16x32_bf16 a[76:79], a[132:135], v[20:23], 0\n"
        "s_add_u32 s82, s81, 147456\n"
        "s_cmp_eq_u32 s82, s56\n"
        "s_cselect_b32 s82, s57, s82\n"
        "v_cndmask_b32_dpp v32, v40, v192, vcc row_ror:8 row_mask:0xf bank_mask:0xf\n"
        "s_add_u32 m0, s54, 57344\n"
        "v_mfma_f32_16x16x32_bf16 a[84:87], a[136:139], v[20:23], 0\n"
        "v_mov_b32_dpp v40, v192 row_ror:8 row_mask:0xf bank_mask:0x3\n"
        "v_fma_mix_f32 v216, v216, v8, v10 op_sel_hi:[1,0,0]\n"
        "buffer_load_dwordx4 v5, s[36:39], s61 offen  lds\n"
        "v_mfma_f32_16x16x32_bf16 a[92:95], a[140:143], v[20:23], 0\n"
        "v_fma_mix_f32 v220, v220, v8, v10 op_sel_hi:[1,0,0]\n"
        "s_add_u32 s83, s82, 147456\n"
        "s_cmp_eq_u32 s83, s56\n"
        "s_cselect_b32 s83, s57, s83\n"
        "v_fma_mix_f32 v218, v218, v9, v11 op_sel_hi:[1,0,0]\n"
        "s_waitcnt vmcnt(11)\n"
        "s_barrier\n"
        "v_mfma_f32_16x16x32_bf16 a[4:7], a[144:147], v[28:31], a[4:7]\n"
        "v_add_u32 v7, 24576, v7\n"
        "s_add_u32 m0, s54, 61440\n"
        "v_fma_mix_f32 v222, v222, v9, v11 op_sel_hi:[1,0,0]\n"
        "v_mfma_f32_16x16x32_bf16 a[12:15], a[148:151], v[28:31], a[12:15]\n"
        "ds_read_b128 a[96:99], v7 offset:0\n"
        "v_cvt_pk_bf16_f32 v208, v208, v212\n"
        "s_add_u32 s84, s83, 147456\n"
        "s_cmp_eq_u32 s84, s56\n"
        "s_cselect_b32 s84, s57, s84\n"
        "v_mfma_f32_16x16x32_bf16 a[20:23], a[152:155], v[28:31], a[20:23]\n"
        "ds_read_b128 a[100:103], v7 offset:2048\n"
        "v_cvt_pk_bf16_f32 v42, v210, v214\n"
        "buffer_load_dwordx4 v5, s[36:39], s62 offen  lds\n"
        "v_mfma_f32_16x16x32_bf16 a[28:31], a[156:159], v[28:31], a[28:31]\n"
        "ds_read_b128 a[104:107], v7 offset:4096\n"
        "v_cndmask_b32_dpp v33, v41, v200, vcc row_ror:8 row_mask:0xf bank_mask:0xf\n"
        "s_add_u32 m0, s54, 65536\n"
        "v_mfma_f32_16x16x32_bf16 a[36:39], a[160:163], v[28:31], a[36:39]\n"
        "ds_read_b128 a[108:111], v7 offset:6144\n"
        "v_mov_b32_dpp v41, v200 row_ror:8 row_mask:0xf bank_mask:0x3\n"
        "ds_read_b128 a[112:115], v7 offset:8192\n"
        "v_mfma_f32_16x16x32_bf16 a[44:47], a[164:167], v[28:31], a[44:47]\n"
        "s_add_u32 s85, s84, 147456\n"
        "s_cmp_eq_u32 s85, s56\n"
        "s_cselect_b32 s85, s57, s85\n"
        "ds_read_b128 a[116:119], v7 offset:10240\n"
        "v_cvt_pk_bf16_f32 v216, v216, v220\n"
        "v_mfma_f32_16x16x32_bf16 a[52:55], a[168:171], v[28:31], a[52:55]\n"
        "buffer_load_dwordx4 v5, s[36:39], s63 offen  lds\n"
        "ds_read_b128 a[120:123], v7 offset:12288\n"
        "v_cvt_pk_bf16_f32 v43, v218, v222\n"
        "v_mfma_f32_16x16x32_bf16 a[60:63], a[172:175], v[28:31], a[60:63]\n"
        "ds_read_b128 a[124:127], v7 offset:14336\n"
        "s_add_u32 s86, s85, 147456\n"
        "s_cmp_eq_u32 s86, s56\n"
        "s_cselect_b32 s86, s57, s86\n"
        "v_cndmask_b32_dpp v34, v42, v208, vcc row_ror:8 row_mask:0xf bank_mask:0xf\n"
        "v_mfma_f32_16x16x32_bf16 a[68:71], a[176:179], v[28:31], a[68:71]\n"
        "ds_read_b128 a[128:131], v7 offset:16384\n"
        "s_add_u32 m0, s54, 69632\n"
        "v_mov_b32_dpp v42, v208 row_ror:8 row_mask:0xf bank_mask:0x3\n"
        "v_mfma_f32_16x16x32_bf16 a[76:79], a[180:183], v[28:31], a[76:79]\n"
        "ds_read_b128 a[132:135], v7 offset:18432\n"
        "s_add_u32 s87, s86, 147456\n"
        "s_cmp_eq_u32 s87, s56\n"
        "s_cselect_b32 s87, s57, s87\n"
        "v_cndmask_b32_dpp v35, v43, v216, vcc row_ror:8 row_mask:0xf bank_mask:0xf\n"
        "v_mfma_f32_16x16x32_bf16 a[84:87], a[184:187], v[28:31], a[84:87]\n"
        "ds_read_b128 a[136:139], v7 offset:20480\n"
        "v_mov_b32_dpp v43, v216 row_ror:8 row_mask:0xf bank_mask:0x3\n"
        "buffer_load_dwordx4 v5, s[36:39], s64 offen  lds\n"
        "v_mfma_f32_16x16x32_bf16 a[92:95], a[188:191], v[28:31], a[92:95]\n"
        "ds_read_b128 a[140:143], v7 offset:22528\n"
        "s_waitcnt lgkmcnt(6)\n"
        "v_mfma_f32_16x16x32_bf16 a[0:3], a[96:99], v[32:35], a[0:3]\n"
        "v_add_u32 v0, 24576, v0\n"
        "ds_read_b128 v[8:11], v6 offset:1024\n"
        "v_fma_mix_f32 v193, v193, v12, v14 op_sel_hi:[1,0,0]\n"
        "v_mfma_f32_16x16x32_bf16 a[8:11], a[100:103], v[32:35], a[8:11]\n"
        "ds_read_b128 a[144:147], v0 offset:0\n"
        "s_mul_i32 s33, s47, 9216\n"
        "v_mfma_f32_16x16x32_bf16 a[16:19], a[104:107], v[32:35], a[16:19]\n"
        "v_fma_mix_f32 v197, v197, v12, v14 op_sel_hi:[1,0,0]\n"
        "ds_read_b128 a[148:151], v0 offset:2048\n"
        "v_fma_mix_f32 v195, v195, v13, v15 op_sel_hi:[1,0,0]\n"
        "v_mfma_f32_16x16x32_bf16 a[24:27], a[108:111], v[32:35], a[24:27]\n"
        "ds_read_b128 a[152:155], v0 offset:4096\n"
        "ds_read_b128 a[156:159], v0 offset:6144\n"
        "v_mfma_f32_16x16x32_bf16 a[32:35], a[112:115], v[32:35], a[32:35]\n"
        "v_fma_mix_f32 v199, v199, v13, v15 op_sel_hi:[1,0,0]\n"
        "s_add_u32 s34, s33, 73728\n"
        "v_mfma_f32_16x16x32_bf16 a[40:43], a[116:119], v[32:35], a[40:43]\n"
        "ds_read_b128 a[160:163], v0 offset:8192\n"
        "v_fma_mix_f32 v201, v201, v12, v14 op_sel_hi:[1,0,0]\n"
        "s_waitcnt lgkmcnt(6)\n"
        "v_mfma_f32_16x16x32_bf16 a[48:51], a[120:123], v[32:35], a[48:51]\n"
        "ds_read_b128 a[164:167], v0 offset:10240\n"
        "v_fma_mix_f32 v205, v205, v12, v14 op_sel_hi:[1,0,0]\n"
        "ds_read_b128 a[168:171], v0 offset:12288\n"
        "v_mfma_f32_16x16x32_bf16 a[56:59], a[124:127], v[32:35], a[56:59]\n"
        "buffer_load_dwordx4 v[160:163], v1, s[28:31], s33 offen\n"
        "v_fma_mix_f32 v203, v203, v13, v15 op_sel_hi:[1,0,0]\n"
        "v_mfma_f32_16x16x32_bf16 a[64:67], a[128:131], v[32:35], a[64:67]\n"
        "ds_read_b128 a[172:175], v0 offset:14336\n"
        "ds_read_b128 a[176:179], v0 offset:16384\n"
        "v_mfma_f32_16x16x32_bf16 a[72:75], a[132:135], v[32:35], a[72:75]\n"
        "v_fma_mix_f32 v207, v207, v13, v15 op_sel_hi:[1,0,0]\n"
        "ds_read_b128 a[180:183], v0 offset:18432\n"
        "v_mfma_f32_16x16x32_bf16 a[80:83], a[136:139], v[32:35], a[80:83]\n"
        "v_cvt_pk_bf16_f32 v193, v193, v197\n"
        "buffer_load_dwordx4 v[164:167], v2, s[28:31], s33 offen\n"
        "ds_read_b128 a[184:187], v0 offset:20480\n"
        "v_mfma_f32_16x16x32_bf16 a[88:91], a[140:143], v[32:35], a[88:91]\n"
        "v_cvt_pk_bf16_f32 v44, v195, v199\n"
        "ds_read_b128 a[188:191], v0 offset:22528\n"
        "s_waitcnt lgkmcnt(6)\n"
        "v_mfma_f32_16x16x32_bf16 a[0:3], a[144:147], v[40:43], a[0:3]\n"
        "v_fma_mix_f32 v209, v209, v12, v14 op_sel_hi:[1,0,0]\n"
        "v_fma_mix_f32 v213, v213, v12, v14 op_sel_hi:[1,0,0]\n"
        "v_mfma_f32_16x16x32_bf16 a[8:11], a[148:151], v[40:43], a[8:11]\n"
        "buffer_load_dwordx4 v[168:171], v3, s[28:31], s33 offen\n"
        "v_fma_mix_f32 v211, v211, v13, v15 op_sel_hi:[1,0,0]\n"
        "v_fma_mix_f32 v215, v215, v13, v15 op_sel_hi:[1,0,0]\n"
        "v_mfma_f32_16x16x32_bf16 a[16:19], a[152:155], v[40:43], a[16:19]\n"
        "v_cvt_pk_bf16_f32 v201, v201, v205\n"
        "v_cvt_pk_bf16_f32 v45, v203, v207\n"
        "v_mfma_f32_16x16x32_bf16 a[24:27], a[156:159], v[40:43], a[24:27]\n"
        "buffer_load_dwordx4 v[172:175], v4, s[28:31], s33 offen\n"
        "v_cndmask_b32_dpp v36, v44, v193, vcc row_ror:8 row_mask:0xf bank_mask:0xf\n"
        "v_mfma_f32_16x16x32_bf16 a[32:35], a[160:163], v[40:43], a[32:35]\n"
        "v_mov_b32_dpp v44, v193 row_ror:8 row_mask:0xf bank_mask:0x3\n"
        "v_fma_mix_f32 v217, v217, v12, v14 op_sel_hi:[1,0,0]\n"
        "buffer_load_dwordx4 v[176:179], v1, s[28:31], s34 offen\n"
        "v_mfma_f32_16x16x32_bf16 a[40:43], a[164:167], v[40:43], a[40:43]\n"
        "v_fma_mix_f32 v221, v221, v12, v14 op_sel_hi:[1,0,0]\n"
        "v_fma_mix_f32 v219, v219, v13, v15 op_sel_hi:[1,0,0]\n"
        "s_waitcnt lgkmcnt(0)\n"
        "v_mfma_f32_16x16x32_bf16 a[48:51], a[168:171], v[40:43], a[48:51]\n"
        "v_fma_mix_f32 v223, v223, v13, v15 op_sel_hi:[1,0,0]\n"
        "v_cvt_pk_bf16_f32 v209, v209, v213\n"
        "v_mfma_f32_16x16x32_bf16 a[56:59], a[172:175], v[40:43], a[56:59]\n"
        "buffer_load_dwordx4 v[180:183], v2, s[28:31], s34 offen\n"
        "v_cvt_pk_bf16_f32 v46, v211, v215\n"
        "v_cndmask_b32_dpp v37, v45, v201, vcc row_ror:8 row_mask:0xf bank_mask:0xf\n"
        "v_mfma_f32_16x16x32_bf16 a[64:67], a[176:179], v[40:43], a[64:67]\n"
        "v_mov_b32_dpp v45, v201 row_ror:8 row_mask:0xf bank_mask:0x3\n"
        "v_cvt_pk_bf16_f32 v217, v217, v221\n"
        "v_mfma_f32_16x16x32_bf16 a[72:75], a[180:183], v[40:43], a[72:75]\n"
        "buffer_load_dwordx4 v[184:187], v3, s[28:31], s34 offen\n"
        "v_cvt_pk_bf16_f32 v47, v219, v223\n"
        "v_mfma_f32_16x16x32_bf16 a[80:83], a[184:187], v[40:43], a[80:83]\n"
        "v_cndmask_b32_dpp v38, v46, v209, vcc row_ror:8 row_mask:0xf bank_mask:0xf\n"
        "v_mov_b32_dpp v46, v209 row_ror:8 row_mask:0xf bank_mask:0x3\n"
        "buffer_load_dwordx4 v[188:191], v4, s[28:31], s34 offen\n"
        "v_mfma_f32_16x16x32_bf16 a[88:91], a[188:191], v[40:43], a[88:91]\n"
        "v_cndmask_b32_dpp v39, v47, v217, vcc row_ror:8 row_mask:0xf bank_mask:0xf\n"
        "v_mov_b32_dpp v47, v217 row_ror:8 row_mask:0xf bank_mask:0x3\n"
        "s_nop 0\n"
        "v_mfma_f32_16x16x32_bf16 a[4:7], a[96:99], v[36:39], a[4:7]\n"
        "ds_read_b128 v[12:15], v6 offset:1280\n"
        "s_sub_u32 s35, s47, s46\n"
        "v_mfma_f32_16x16x32_bf16 a[12:15], a[100:103], v[36:39], a[12:15]\n"
        "s_waitcnt vmcnt(18)\n"
        "v_fma_mix_f32 v224, v224, v8, v10 op_sel_hi:[1,0,0]\n"
        "v_fma_mix_f32 v228, v228, v8, v10 op_sel_hi:[1,0,0]\n"
        "v_fma_mix_f32 v226, v226, v9, v11 op_sel_hi:[1,0,0]\n"
        "v_mfma_f32_16x16x32_bf16 a[20:23], a[104:107], v[36:39], a[20:23]\n"
        "v_add_u32 v5, s35, v5\n"
        "v_fma_mix_f32 v230, v230, v9, v11 op_sel_hi:[1,0,0]\n"
        "v_mfma_f32_16x16x32_bf16 a[28:31], a[108:111], v[36:39], a[28:31]\n"
        "v_fma_mix_f32 v232, v232, v8, v10 op_sel_hi:[1,0,0]\n"
        "v_fma_mix_f32 v236, v236, v8, v10 op_sel_hi:[1,0,0]\n"
        "s_add_u32 m0, s54, 73728\n"
        "v_mfma_f32_16x16x32_bf16 a[36:39], a[112:115], v[36:39], a[36:39]\n"
        "v_fma_mix_f32 v234, v234, v9, v11 op_sel_hi:[1,0,0]\n"
        "v_fma_mix_f32 v238, v238, v9, v11 op_sel_hi:[1,0,0]\n"
        "v_mfma_f32_16x16x32_bf16 a[44:47], a[116:119], v[36:39], a[44:47]\n"
        "v_cvt_pk_bf16_f32 v224, v224, v228\n"
        "buffer_load_dwordx4 v5, s[36:39], s59 offen  lds\n"
        "v_cvt_pk_bf16_f32 v56, v226, v230\n"
        "v_mfma_f32_16x16x32_bf16 a[52:55], a[120:123], v[36:39], a[52:55]\n"
        "s_waitcnt vmcnt(15)\n"
        "v_fma_mix_f32 v240, v240, v8, v10 op_sel_hi:[1,0,0]\n"
        "s_add_u32 m0, s54, 77824\n"
        "v_mfma_f32_16x16x32_bf16 a[60:63], a[124:127], v[36:39], a[60:63]\n"
        "v_fma_mix_f32 v244, v244, v8, v10 op_sel_hi:[1,0,0]\n"
        "v_fma_mix_f32 v242, v242, v9, v11 op_sel_hi:[1,0,0]\n"
        "v_fma_mix_f32 v246, v246, v9, v11 op_sel_hi:[1,0,0]\n"
        "v_mfma_f32_16x16x32_bf16 a[68:71], a[128:131], v[36:39], a[68:71]\n"
        "buffer_load_dwordx4 v5, s[36:39], s60 offen  lds\n"
        "v_cvt_pk_bf16_f32 v232, v232, v236\n"
        "v_mfma_f32_16x16x32_bf16 a[76:79], a[132:135], v[36:39], a[76:79]\n"
        "v_cvt_pk_bf16_f32 v57, v234, v238\n"
        "v_cndmask_b32_dpp v48, v56, v224, vcc row_ror:8 row_mask:0xf bank_mask:0xf\n"
        "s_add_u32 m0, s54, 81920\n"
        "v_mfma_f32_16x16x32_bf16 a[84:87], a[136:139], v[36:39], a[84:87]\n"
        "v_mov_b32_dpp v56, v224 row_ror:8 row_mask:0xf bank_mask:0x3\n"
        "v_fma_mix_f32 v248, v248, v8, v10 op_sel_hi:[1,0,0]\n"
        "v_mfma_f32_16x16x32_bf16 a[92:95], a[140:143], v[36:39], a[92:95]\n"
        "v_fma_mix_f32 v252, v252, v8, v10 op_sel_hi:[1,0,0]\n"
        "buffer_load_dwordx4 v5, s[36:39], s61 offen  lds\n"
        "v_fma_mix_f32 v250, v250, v9, v11 op_sel_hi:[1,0,0]\n"
        "s_waitcnt vmcnt(11)\n"
        "s_barrier\n"
        "v_mfma_f32_16x16x32_bf16 a[4:7], a[144:147], v[44:47], a[4:7]\n"
        "v_add_u32 v7, 24576, v7\n"
        "v_fma_mix_f32 v254, v254, v9, v11 op_sel_hi:[1,0,0]\n"
        "s_add_u32 m0, s54, 86016\n"
        "v_mfma_f32_16x16x32_bf16 a[12:15], a[148:151], v[44:47], a[12:15]\n"
        "ds_read_b128 a[96:99], v7 offset:0\n"
        "v_cvt_pk_bf16_f32 v240, v240, v244\n"
        "v_mfma_f32_16x16x32_bf16 a[20:23], a[152:155], v[44:47], a[20:23]\n"
        "ds_read_b128 a[100:103], v7 offset:2048\n"
        "v_cvt_pk_bf16_f32 v58, v242, v246\n"
        "buffer_load_dwordx4 v5, s[36:39], s62 offen  lds\n"
        "v_mfma_f32_16x16x32_bf16 a[28:31], a[156:159], v[44:47], a[28:31]\n"
        "ds_read_b128 a[104:107], v7 offset:4096\n"
        "v_cndmask_b32_dpp v49, v57, v232, vcc row_ror:8 row_mask:0xf bank_mask:0xf\n"
        "v_mfma_f32_16x16x32_bf16 a[36:39], a[160:163], v[44:47], a[36:39]\n"
        "ds_read_b128 a[108:111], v7 offset:6144\n"
        "v_mov_b32_dpp v57, v232 row_ror:8 row_mask:0xf bank_mask:0x3\n"
        "s_add_u32 m0, s54, 90112\n"
        "v_mfma_f32_16x16x32_bf16 a[44:47], a[164:167], v[44:47], a[44:47]\n"
        "ds_read_b128 a[112:115], v7 offset:8192\n"
        "ds_read_b128 a[116:119], v7 offset:10240\n"
        "v_cvt_pk_bf16_f32 v248, v248, v252\n"
        "v_mfma_f32_16x16x32_bf16 a[52:55], a[168:171], v[44:47], a[52:55]\n"
        "ds_read_b128 a[120:123], v7 offset:12288\n"
        "buffer_load_dwordx4 v5, s[36:39], s63 offen  lds\n"
        "v_mfma_f32_16x16x32_bf16 a[60:63], a[172:175], v[44:47], a[60:63]\n"
        "v_cvt_pk_bf16_f32 v59, v250, v254\n"
        "ds_read_b128 a[124:127], v7 offset:14336\n"
        "v_cndmask_b32_dpp v50, v58, v240, vcc row_ror:8 row_mask:0xf bank_mask:0xf\n"
        "v_mfma_f32_16x16x32_bf16 a[68:71], a[176:179], v[44:47], a[68:71]\n"
        "ds_read_b128 a[128:131], v7 offset:16384\n"
        "s_add_u32 m0, s54, 94208\n"
        "v_mfma_f32_16x16x32_bf16 a[76:79], a[180:183], v[44:47], a[76:79]\n"
        "v_mov_b32_dpp v58, v240 row_ror:8 row_mask:0xf bank_mask:0x3\n"
        "ds_read_b128 a[132:135], v7 offset:18432\n"
        "v_cndmask_b32_dpp v51, v59, v248, vcc row_ror:8 row_mask:0xf bank_mask:0xf\n"
        "v_mfma_f32_16x16x32_bf16 a[84:87], a[184:187], v[44:47], a[84:87]\n"
        "ds_read_b128 a[136:139], v7 offset:20480\n"
        "buffer_load_dwordx4 v5, s[36:39], s64 offen  lds\n"
        "v_mov_b32_dpp v59, v248 row_ror:8 row_mask:0xf bank_mask:0x3\n"
        "v_mfma_f32_16x16x32_bf16 a[92:95], a[188:191], v[44:47], a[92:95]\n"
        "ds_read_b128 a[140:143], v7 offset:22528\n"
        "s_waitcnt lgkmcnt(6)\n"
        "v_mfma_f32_16x16x32_bf16 a[0:3], a[96:99], v[48:51], a[0:3]\n"
        "v_add_u32 v0, 24576, v0\n"
        "ds_read_b128 v[8:11], v6 offset:1536\n"
        "v_fma_mix_f32 v225, v225, v12, v14 op_sel_hi:[1,0,0]\n"
        "v_mfma_f32_16x16x32_bf16 a[8:11], a[100:103], v[48:51], a[8:11]\n"
        "ds_read_b128 a[144:147], v0 offset:0\n"
        "s_mul_i32 s33, s48, 9216\n"
        "v_mfma_f32_16x16x32_bf16 a[16:19], a[104:107], v[48:51], a[16:19]\n"
        "v_fma_mix_f32 v229, v229, v12, v14 op_sel_hi:[1,0,0]\n"
        "ds_read_b128 a[148:151], v0 offset:2048\n"
        "v_fma_mix_f32 v227, v227, v13, v15 op_sel_hi:[1,0,0]\n"
        "v_mfma_f32_16x16x32_bf16 a[24:27], a[108:111], v[48:51], a[24:27]\n"
        "ds_read_b128 a[152:155], v0 offset:4096\n"
        "ds_read_b128 a[156:159], v0 offset:6144\n"
        "v_mfma_f32_16x16x32_bf16 a[32:35], a[112:115], v[48:51], a[32:35]\n"
        "v_fma_mix_f32 v231, v231, v13, v15 op_sel_hi:[1,0,0]\n"
        "s_add_u32 s34, s33, 73728\n"
        "v_mfma_f32_16x16x32_bf16 a[40:43], a[116:119], v[48:51], a[40:43]\n"
        "ds_read_b128 a[160:163], v0 offset:8192\n"
        "v_fma_mix_f32 v233, v233, v12, v14 op_sel_hi:[1,0,0]\n"
        "s_waitcnt lgkmcnt(6)\n"
        "v_mfma_f32_16x16x32_bf16 a[48:51], a[120:123], v[48:51], a[48:51]\n"
        "ds_read_b128 a[164:167], v0 offset:10240\n"
        "v_fma_mix_f32 v237, v237, v12, v14 op_sel_hi:[1,0,0]\n"
        "ds_read_b128 a[168:171], v0 offset:12288\n"
        "v_mfma_f32_16x16x32_bf16 a[56:59], a[124:127], v[48:51], a[56:59]\n"
        "buffer_load_dwordx4 v[192:195], v1, s[28:31], s33 offen\n"
        "v_fma_mix_f32 v235, v235, v13, v15 op_sel_hi:[1,0,0]\n"
        "v_mfma_f32_16x16x32_bf16 a[64:67], a[128:131], v[48:51], a[64:67]\n"
        "ds_read_b128 a[172:175], v0 offset:14336\n"
        "ds_read_b128 a[176:179], v0 offset:16384\n"
        "v_mfma_f32_16x16x32_bf16 a[72:75], a[132:135], v[48:51], a[72:75]\n"
        "v_fma_mix_f32 v239, v239, v13, v15 op_sel_hi:[1,0,0]\n"
        "ds_read_b128 a[180:183], v0 offset:18432\n"
        "v_mfma_f32_16x16x32_bf16 a[80:83], a[136:139], v[48:51], a[80:83]\n"
        "v_cvt_pk_bf16_f32 v225, v225, v229\n"
        "buffer_load_dwordx4 v[196:199], v2, s[28:31], s33 offen\n"
        "ds_read_b128 a[184:187], v0 offset:20480\n"
        "v_mfma_f32_16x16x32_bf16 a[88:91], a[140:143], v[48:51], a[88:91]\n"
        "v_cvt_pk_bf16_f32 v60, v227, v231\n"
        "ds_read_b128 a[188:191], v0 offset:22528\n"
        "s_waitcnt lgkmcnt(6)\n"
        "v_mfma_f32_16x16x32_bf16 a[0:3], a[144:147], v[56:59], a[0:3]\n"
        "v_fma_mix_f32 v241, v241, v12, v14 op_sel_hi:[1,0,0]\n"
        "v_fma_mix_f32 v245, v245, v12, v14 op_sel_hi:[1,0,0]\n"
        "v_mfma_f32_16x16x32_bf16 a[8:11], a[148:151], v[56:59], a[8:11]\n"
        "buffer_load_dwordx4 v[200:203], v3, s[28:31], s33 offen\n"
        "v_fma_mix_f32 v243, v243, v13, v15 op_sel_hi:[1,0,0]\n"
        "v_fma_mix_f32 v247, v247, v13, v15 op_sel_hi:[1,0,0]\n"
        "v_mfma_f32_16x16x32_bf16 a[16:19], a[152:155], v[56:59], a[16:19]\n"
        "v_cvt_pk_bf16_f32 v233, v233, v237\n"
        "v_cvt_pk_bf16_f32 v61, v235, v239\n"
        "v_mfma_f32_16x16x32_bf16 a[24:27], a[156:159], v[56:59], a[24:27]\n"
        "buffer_load_dwordx4 v[204:207], v4, s[28:31], s33 offen\n"
        "v_cndmask_b32_dpp v52, v60, v225, vcc row_ror:8 row_mask:0xf bank_mask:0xf\n"
        "v_mfma_f32_16x16x32_bf16 a[32:35], a[160:163], v[56:59], a[32:35]\n"
        "v_mov_b32_dpp v60, v225 row_ror:8 row_mask:0xf bank_mask:0x3\n"
        "v_fma_mix_f32 v249, v249, v12, v14 op_sel_hi:[1,0,0]\n"
        "buffer_load_dwordx4 v[208:211], v1, s[28:31], s34 offen\n"
        "v_mfma_f32_16x16x32_bf16 a[40:43], a[164:167], v[56:59], a[40:43]\n"
        "v_fma_mix_f32 v253, v253, v12, v14 op_sel_hi:[1,0,0]\n"
        "v_fma_mix_f32 v251, v251, v13, v15 op_sel_hi:[1,0,0]\n"
        "s_waitcnt lgkmcnt(0)\n"
        "v_mfma_f32_16x16x32_bf16 a[48:51], a[168:171], v[56:59], a[48:51]\n"
        "v_fma_mix_f32 v255, v255, v13, v15 op_sel_hi:[1,0,0]\n"
        "v_cvt_pk_bf16_f32 v241, v241, v245\n"
        "v_mfma_f32_16x16x32_bf16 a[56:59], a[172:175], v[56:59], a[56:59]\n"
        "buffer_load_dwordx4 v[212:215], v2, s[28:31], s34 offen\n"
        "v_cvt_pk_bf16_f32 v62, v243, v247\n"
        "v_cndmask_b32_dpp v53, v61, v233, vcc row_ror:8 row_mask:0xf bank_mask:0xf\n"
        "v_mfma_f32_16x16x32_bf16 a[64:67], a[176:179], v[56:59], a[64:67]\n"
        "v_mov_b32_dpp v61, v233 row_ror:8 row_mask:0xf bank_mask:0x3\n"
        "v_cvt_pk_bf16_f32 v249, v249, v253\n"
        "v_mfma_f32_16x16x32_bf16 a[72:75], a[180:183], v[56:59], a[72:75]\n"
        "buffer_load_dwordx4 v[216:219], v3, s[28:31], s34 offen\n"
        "v_cvt_pk_bf16_f32 v63, v251, v255\n"
        "v_mfma_f32_16x16x32_bf16 a[80:83], a[184:187], v[56:59], a[80:83]\n"
        "v_cndmask_b32_dpp v54, v62, v241, vcc row_ror:8 row_mask:0xf bank_mask:0xf\n"
        "v_mov_b32_dpp v62, v241 row_ror:8 row_mask:0xf bank_mask:0x3\n"
        "buffer_load_dwordx4 v[220:223], v4, s[28:31], s34 offen\n"
        "v_mfma_f32_16x16x32_bf16 a[88:91], a[188:191], v[56:59], a[88:91]\n"
        "v_cndmask_b32_dpp v55, v63, v249, vcc row_ror:8 row_mask:0xf bank_mask:0xf\n"
        "v_mov_b32_dpp v63, v249 row_ror:8 row_mask:0xf bank_mask:0x3\n"
        "s_nop 0\n"
        "v_mfma_f32_16x16x32_bf16 a[4:7], a[96:99], v[52:55], a[4:7]\n"
        "ds_read_b128 v[12:15], v6 offset:1792\n"
        "s_sub_u32 s35, s48, s47\n"
        "v_mfma_f32_16x16x32_bf16 a[12:15], a[100:103], v[52:55], a[12:15]\n"
        "s_waitcnt vmcnt(18)\n"
        "v_fma_mix_f32 v160, v160, v8, v10 op_sel_hi:[1,0,0]\n"
        "v_fma_mix_f32 v164, v164, v8, v10 op_sel_hi:[1,0,0]\n"
        "v_fma_mix_f32 v162, v162, v9, v11 op_sel_hi:[1,0,0]\n"
        "v_mfma_f32_16x16x32_bf16 a[20:23], a[104:107], v[52:55], a[20:23]\n"
        "v_add_u32 v5, s35, v5\n"
        "v_fma_mix_f32 v166, v166, v9, v11 op_sel_hi:[1,0,0]\n"
        "v_mfma_f32_16x16x32_bf16 a[28:31], a[108:111], v[52:55], a[28:31]\n"
        "v_fma_mix_f32 v168, v168, v8, v10 op_sel_hi:[1,0,0]\n"
        "v_fma_mix_f32 v172, v172, v8, v10 op_sel_hi:[1,0,0]\n"
        "s_add_u32 m0, s54, 0\n"
        "v_mfma_f32_16x16x32_bf16 a[36:39], a[112:115], v[52:55], a[36:39]\n"
        "v_fma_mix_f32 v170, v170, v9, v11 op_sel_hi:[1,0,0]\n"
        "v_fma_mix_f32 v174, v174, v9, v11 op_sel_hi:[1,0,0]\n"
        "v_mfma_f32_16x16x32_bf16 a[44:47], a[116:119], v[52:55], a[44:47]\n"
        "v_cvt_pk_bf16_f32 v160, v160, v164\n"
        "buffer_load_dwordx4 v5, s[36:39], s59 offen  lds\n"
        "v_cvt_pk_bf16_f32 v72, v162, v166\n"
        "v_mfma_f32_16x16x32_bf16 a[52:55], a[120:123], v[52:55], a[52:55]\n"
        "s_waitcnt vmcnt(15)\n"
        "v_fma_mix_f32 v176, v176, v8, v10 op_sel_hi:[1,0,0]\n"
        "s_add_u32 m0, s54, 4096\n"
        "v_mfma_f32_16x16x32_bf16 a[60:63], a[124:127], v[52:55], a[60:63]\n"
        "v_fma_mix_f32 v180, v180, v8, v10 op_sel_hi:[1,0,0]\n"
        "v_fma_mix_f32 v178, v178, v9, v11 op_sel_hi:[1,0,0]\n"
        "v_fma_mix_f32 v182, v182, v9, v11 op_sel_hi:[1,0,0]\n"
        "v_mfma_f32_16x16x32_bf16 a[68:71], a[128:131], v[52:55], a[68:71]\n"
        "buffer_load_dwordx4 v5, s[36:39], s60 offen  lds\n"
        "v_cvt_pk_bf16_f32 v168, v168, v172\n"
        "v_mfma_f32_16x16x32_bf16 a[76:79], a[132:135], v[52:55], a[76:79]\n"
        "v_cvt_pk_bf16_f32 v73, v170, v174\n"
        "v_cndmask_b32_dpp v64, v72, v160, vcc row_ror:8 row_mask:0xf bank_mask:0xf\n"
        "s_add_u32 m0, s54, 8192\n"
        "v_mfma_f32_16x16x32_bf16 a[84:87], a[136:139], v[52:55], a[84:87]\n"
        "v_mov_b32_dpp v72, v160 row_ror:8 row_mask:0xf bank_mask:0x3\n"
        "v_fma_mix_f32 v184, v184, v8, v10 op_sel_hi:[1,0,0]\n"
        "v_mfma_f32_16x16x32_bf16 a[92:95], a[140:143], v[52:55], a[92:95]\n"
        "v_fma_mix_f32 v188, v188, v8, v10 op_sel_hi:[1,0,0]\n"
        "buffer_load_dwordx4 v5, s[36:39], s61 offen  lds\n"
        "v_fma_mix_f32 v186, v186, v9, v11 op_sel_hi:[1,0,0]\n"
        "s_waitcnt vmcnt(11)\n"
        "s_barrier\n"
        "v_mfma_f32_16x16x32_bf16 a[4:7], a[144:147], v[60:63], a[4:7]\n"
        "v_add_u32 v7, 24576, v7\n"
        "v_fma_mix_f32 v190, v190, v9, v11 op_sel_hi:[1,0,0]\n"
        "s_add_u32 m0, s54, 12288\n"
        "v_mfma_f32_16x16x32_bf16 a[12:15], a[148:151], v[60:63], a[12:15]\n"
        "ds_read_b128 a[96:99], v7 offset:0\n"
        "v_cvt_pk_bf16_f32 v176, v176, v180\n"
        "v_mfma_f32_16x16x32_bf16 a[20:23], a[152:155], v[60:63], a[20:23]\n"
        "ds_read_b128 a[100:103], v7 offset:2048\n"
        "v_cvt_pk_bf16_f32 v74, v178, v182\n"
        "buffer_load_dwordx4 v5, s[36:39], s62 offen  lds\n"
        "v_mfma_f32_16x16x32_bf16 a[28:31], a[156:159], v[60:63], a[28:31]\n"
        "ds_read_b128 a[104:107], v7 offset:4096\n"
        "v_cndmask_b32_dpp v65, v73, v168, vcc row_ror:8 row_mask:0xf bank_mask:0xf\n"
        "v_mfma_f32_16x16x32_bf16 a[36:39], a[160:163], v[60:63], a[36:39]\n"
        "ds_read_b128 a[108:111], v7 offset:6144\n"
        "v_mov_b32_dpp v73, v168 row_ror:8 row_mask:0xf bank_mask:0x3\n"
        "s_add_u32 m0, s54, 16384\n"
        "v_mfma_f32_16x16x32_bf16 a[44:47], a[164:167], v[60:63], a[44:47]\n"
        "ds_read_b128 a[112:115], v7 offset:8192\n"
        "ds_read_b128 a[116:119], v7 offset:10240\n"
        "v_cvt_pk_bf16_f32 v184, v184, v188\n"
        "v_mfma_f32_16x16x32_bf16 a[52:55], a[168:171], v[60:63], a[52:55]\n"
        "ds_read_b128 a[120:123], v7 offset:12288\n"
        "buffer_load_dwordx4 v5, s[36:39], s63 offen  lds\n"
        "v_mfma_f32_16x16x32_bf16 a[60:63], a[172:175], v[60:63], a[60:63]\n"
        "v_cvt_pk_bf16_f32 v75, v186, v190\n"
        "ds_read_b128 a[124:127], v7 offset:14336\n"
        "v_cndmask_b32_dpp v66, v74, v176, vcc row_ror:8 row_mask:0xf bank_mask:0xf\n"
        "v_mfma_f32_16x16x32_bf16 a[68:71], a[176:179], v[60:63], a[68:71]\n"
        "ds_read_b128 a[128:131], v7 offset:16384\n"
        "s_add_u32 m0, s54, 20480\n"
        "v_mfma_f32_16x16x32_bf16 a[76:79], a[180:183], v[60:63], a[76:79]\n"
        "v_mov_b32_dpp v74, v176 row_ror:8 row_mask:0xf bank_mask:0x3\n"
        "ds_read_b128 a[132:135], v7 offset:18432\n"
        "v_cndmask_b32_dpp v67, v75, v184, vcc row_ror:8 row_mask:0xf bank_mask:0xf\n"
        "v_mfma_f32_16x16x32_bf16 a[84:87], a[184:187], v[60:63], a[84:87]\n"
        "ds_read_b128 a[136:139], v7 offset:20480\n"
        "buffer_load_dwordx4 v5, s[36:39], s64 offen  lds\n"
        "v_mov_b32_dpp v75, v184 row_ror:8 row_mask:0xf bank_mask:0x3\n"
        "v_mfma_f32_16x16x32_bf16 a[92:95], a[188:191], v[60:63], a[92:95]\n"
        "ds_read_b128 a[140:143], v7 offset:22528\n"
        "s_waitcnt lgkmcnt(6)\n"
        "v_mfma_f32_16x16x32_bf16 a[0:3], a[96:99], v[64:67], a[0:3]\n"
        "v_add_u32 v0, 24576, v0\n"
        "ds_read_b128 v[8:11], v6 offset:2048\n"
        "v_fma_mix_f32 v161, v161, v12, v14 op_sel_hi:[1,0,0]\n"
        "v_mfma_f32_16x16x32_bf16 a[8:11], a[100:103], v[64:67], a[8:11]\n"
        "ds_read_b128 a[144:147], v0 offset:0\n"
        "s_mul_i32 s33, s49, 9216\n"
        "v_mfma_f32_16x16x32_bf16 a[16:19], a[104:107], v[64:67], a[16:19]\n"
        "v_fma_mix_f32 v165, v165, v12, v14 op_sel_hi:[1,0,0]\n"
        "ds_read_b128 a[148:151], v0 offset:2048\n"
        "v_fma_mix_f32 v163, v163, v13, v15 op_sel_hi:[1,0,0]\n"
        "v_mfma_f32_16x16x32_bf16 a[24:27], a[108:111], v[64:67], a[24:27]\n"
        "ds_read_b128 a[152:155], v0 offset:4096\n"
        "ds_read_b128 a[156:159], v0 offset:6144\n"
        "v_mfma_f32_16x16x32_bf16 a[32:35], a[112:115], v[64:67], a[32:35]\n"
        "v_fma_mix_f32 v167, v167, v13, v15 op_sel_hi:[1,0,0]\n"
        "s_add_u32 s34, s33, 73728\n"
        "v_mfma_f32_16x16x32_bf16 a[40:43], a[116:119], v[64:67], a[40:43]\n"
        "ds_read_b128 a[160:163], v0 offset:8192\n"
        "v_fma_mix_f32 v169, v169, v12, v14 op_sel_hi:[1,0,0]\n"
        "s_waitcnt lgkmcnt(6)\n"
        "v_mfma_f32_16x16x32_bf16 a[48:51], a[120:123], v[64:67], a[48:51]\n"
        "ds_read_b128 a[164:167], v0 offset:10240\n"
        "v_fma_mix_f32 v173, v173, v12, v14 op_sel_hi:[1,0,0]\n"
        "ds_read_b128 a[168:171], v0 offset:12288\n"
        "v_mfma_f32_16x16x32_bf16 a[56:59], a[124:127], v[64:67], a[56:59]\n"
        "buffer_load_dwordx4 v[224:227], v1, s[28:31], s33 offen\n"
        "v_fma_mix_f32 v171, v171, v13, v15 op_sel_hi:[1,0,0]\n"
        "v_mfma_f32_16x16x32_bf16 a[64:67], a[128:131], v[64:67], a[64:67]\n"
        "ds_read_b128 a[172:175], v0 offset:14336\n"
        "ds_read_b128 a[176:179], v0 offset:16384\n"
        "v_mfma_f32_16x16x32_bf16 a[72:75], a[132:135], v[64:67], a[72:75]\n"
        "v_fma_mix_f32 v175, v175, v13, v15 op_sel_hi:[1,0,0]\n"
        "ds_read_b128 a[180:183], v0 offset:18432\n"
        "v_mfma_f32_16x16x32_bf16 a[80:83], a[136:139], v[64:67], a[80:83]\n"
        "v_cvt_pk_bf16_f32 v161, v161, v165\n"
        "buffer_load_dwordx4 v[228:231], v2, s[28:31], s33 offen\n"
        "ds_read_b128 a[184:187], v0 offset:20480\n"
        "v_mfma_f32_16x16x32_bf16 a[88:91], a[140:143], v[64:67], a[88:91]\n"
        "v_cvt_pk_bf16_f32 v76, v163, v167\n"
        "ds_read_b128 a[188:191], v0 offset:22528\n"
        "s_waitcnt lgkmcnt(6)\n"
        "v_mfma_f32_16x16x32_bf16 a[0:3], a[144:147], v[72:75], a[0:3]\n"
        "v_fma_mix_f32 v177, v177, v12, v14 op_sel_hi:[1,0,0]\n"
        "v_fma_mix_f32 v181, v181, v12, v14 op_sel_hi:[1,0,0]\n"
        "v_mfma_f32_16x16x32_bf16 a[8:11], a[148:151], v[72:75], a[8:11]\n"
        "buffer_load_dwordx4 v[232:235], v3, s[28:31], s33 offen\n"
        "v_fma_mix_f32 v179, v179, v13, v15 op_sel_hi:[1,0,0]\n"
        "v_fma_mix_f32 v183, v183, v13, v15 op_sel_hi:[1,0,0]\n"
        "v_mfma_f32_16x16x32_bf16 a[16:19], a[152:155], v[72:75], a[16:19]\n"
        "v_cvt_pk_bf16_f32 v169, v169, v173\n"
        "v_cvt_pk_bf16_f32 v77, v171, v175\n"
        "v_mfma_f32_16x16x32_bf16 a[24:27], a[156:159], v[72:75], a[24:27]\n"
        "buffer_load_dwordx4 v[236:239], v4, s[28:31], s33 offen\n"
        "v_cndmask_b32_dpp v68, v76, v161, vcc row_ror:8 row_mask:0xf bank_mask:0xf\n"
        "v_mfma_f32_16x16x32_bf16 a[32:35], a[160:163], v[72:75], a[32:35]\n"
        "v_mov_b32_dpp v76, v161 row_ror:8 row_mask:0xf bank_mask:0x3\n"
        "v_fma_mix_f32 v185, v185, v12, v14 op_sel_hi:[1,0,0]\n"
        "buffer_load_dwordx4 v[240:243], v1, s[28:31], s34 offen\n"
        "v_mfma_f32_16x16x32_bf16 a[40:43], a[164:167], v[72:75], a[40:43]\n"
        "v_fma_mix_f32 v189, v189, v12, v14 op_sel_hi:[1,0,0]\n"
        "v_fma_mix_f32 v187, v187, v13, v15 op_sel_hi:[1,0,0]\n"
        "s_waitcnt lgkmcnt(0)\n"
        "v_mfma_f32_16x16x32_bf16 a[48:51], a[168:171], v[72:75], a[48:51]\n"
        "v_fma_mix_f32 v191, v191, v13, v15 op_sel_hi:[1,0,0]\n"
        "v_cvt_pk_bf16_f32 v177, v177, v181\n"
        "v_mfma_f32_16x16x32_bf16 a[56:59], a[172:175], v[72:75], a[56:59]\n"
        "buffer_load_dwordx4 v[244:247], v2, s[28:31], s34 offen\n"
        "v_cvt_pk_bf16_f32 v78, v179, v183\n"
        "v_cndmask_b32_dpp v69, v77, v169, vcc row_ror:8 row_mask:0xf bank_mask:0xf\n"
        "v_mfma_f32_16x16x32_bf16 a[64:67], a[176:179], v[72:75], a[64:67]\n"
        "v_mov_b32_dpp v77, v169 row_ror:8 row_mask:0xf bank_mask:0x3\n"
        "v_cvt_pk_bf16_f32 v185, v185, v189\n"
        "v_mfma_f32_16x16x32_bf16 a[72:75], a[180:183], v[72:75], a[72:75]\n"
        "buffer_load_dwordx4 v[248:251], v3, s[28:31], s34 offen\n"
        "v_cvt_pk_bf16_f32 v79, v187, v191\n"
        "v_mfma_f32_16x16x32_bf16 a[80:83], a[184:187], v[72:75], a[80:83]\n"
        "v_cndmask_b32_dpp v70, v78, v177, vcc row_ror:8 row_mask:0xf bank_mask:0xf\n"
        "v_mov_b32_dpp v78, v177 row_ror:8 row_mask:0xf bank_mask:0x3\n"
        "buffer_load_dwordx4 v[252:255], v4, s[28:31], s34 offen\n"
        "v_mfma_f32_16x16x32_bf16 a[88:91], a[188:191], v[72:75], a[88:91]\n"
        "v_cndmask_b32_dpp v71, v79, v185, vcc row_ror:8 row_mask:0xf bank_mask:0xf\n"
        "v_mov_b32_dpp v79, v185 row_ror:8 row_mask:0xf bank_mask:0x3\n"
        "s_nop 0\n"
        "v_mfma_f32_16x16x32_bf16 a[4:7], a[96:99], v[68:71], a[4:7]\n"
        "ds_read_b128 v[12:15], v6 offset:2304\n"
        "s_sub_u32 s35, s49, s48\n"
        "v_mfma_f32_16x16x32_bf16 a[12:15], a[100:103], v[68:71], a[12:15]\n"
        "s_waitcnt vmcnt(18)\n"
        "v_fma_mix_f32 v192, v192, v8, v10 op_sel_hi:[1,0,0]\n"
        "v_fma_mix_f32 v196, v196, v8, v10 op_sel_hi:[1,0,0]\n"
        "v_fma_mix_f32 v194, v194, v9, v11 op_sel_hi:[1,0,0]\n"
        "v_mfma_f32_16x16x32_bf16 a[20:23], a[104:107], v[68:71], a[20:23]\n"
        "v_add_u32 v5, s35, v5\n"
        "v_fma_mix_f32 v198, v198, v9, v11 op_sel_hi:[1,0,0]\n"
        "v_mfma_f32_16x16x32_bf16 a[28:31], a[108:111], v[68:71], a[28:31]\n"
        "v_fma_mix_f32 v200, v200, v8, v10 op_sel_hi:[1,0,0]\n"
        "v_fma_mix_f32 v204, v204, v8, v10 op_sel_hi:[1,0,0]\n"
        "s_add_u32 m0, s54, 24576\n"
        "v_mfma_f32_16x16x32_bf16 a[36:39], a[112:115], v[68:71], a[36:39]\n"
        "v_fma_mix_f32 v202, v202, v9, v11 op_sel_hi:[1,0,0]\n"
        "v_fma_mix_f32 v206, v206, v9, v11 op_sel_hi:[1,0,0]\n"
        "v_mfma_f32_16x16x32_bf16 a[44:47], a[116:119], v[68:71], a[44:47]\n"
        "v_cvt_pk_bf16_f32 v192, v192, v196\n"
        "buffer_load_dwordx4 v5, s[36:39], s59 offen  lds\n"
        "v_cvt_pk_bf16_f32 v88, v194, v198\n"
        "v_mfma_f32_16x16x32_bf16 a[52:55], a[120:123], v[68:71], a[52:55]\n"
        "s_waitcnt vmcnt(15)\n"
        "v_fma_mix_f32 v208, v208, v8, v10 op_sel_hi:[1,0,0]\n"
        "s_add_u32 m0, s54, 28672\n"
        "v_mfma_f32_16x16x32_bf16 a[60:63], a[124:127], v[68:71], a[60:63]\n"
        "v_fma_mix_f32 v212, v212, v8, v10 op_sel_hi:[1,0,0]\n"
        "v_fma_mix_f32 v210, v210, v9, v11 op_sel_hi:[1,0,0]\n"
        "v_fma_mix_f32 v214, v214, v9, v11 op_sel_hi:[1,0,0]\n"
        "v_mfma_f32_16x16x32_bf16 a[68:71], a[128:131], v[68:71], a[68:71]\n"
        "buffer_load_dwordx4 v5, s[36:39], s60 offen  lds\n"
        "v_cvt_pk_bf16_f32 v200, v200, v204\n"
        "v_mfma_f32_16x16x32_bf16 a[76:79], a[132:135], v[68:71], a[76:79]\n"
        "v_cvt_pk_bf16_f32 v89, v202, v206\n"
        "v_cndmask_b32_dpp v80, v88, v192, vcc row_ror:8 row_mask:0xf bank_mask:0xf\n"
        "s_add_u32 m0, s54, 32768\n"
        "v_mfma_f32_16x16x32_bf16 a[84:87], a[136:139], v[68:71], a[84:87]\n"
        "v_mov_b32_dpp v88, v192 row_ror:8 row_mask:0xf bank_mask:0x3\n"
        "v_fma_mix_f32 v216, v216, v8, v10 op_sel_hi:[1,0,0]\n"
        "v_mfma_f32_16x16x32_bf16 a[92:95], a[140:143], v[68:71], a[92:95]\n"
        "v_fma_mix_f32 v220, v220, v8, v10 op_sel_hi:[1,0,0]\n"
        "buffer_load_dwordx4 v5, s[36:39], s61 offen  lds\n"
        "v_fma_mix_f32 v218, v218, v9, v11 op_sel_hi:[1,0,0]\n"
        "s_waitcnt vmcnt(11)\n"
        "s_barrier\n"
        "v_mfma_f32_16x16x32_bf16 a[4:7], a[144:147], v[76:79], a[4:7]\n"
        "v_add_u32 v7, -73728, v7\n"
        "v_fma_mix_f32 v222, v222, v9, v11 op_sel_hi:[1,0,0]\n"
        "s_add_u32 m0, s54, 36864\n"
        "v_mfma_f32_16x16x32_bf16 a[12:15], a[148:151], v[76:79], a[12:15]\n"
        "ds_read_b128 a[96:99], v7 offset:0\n"
        "v_cvt_pk_bf16_f32 v208, v208, v212\n"
        "v_mfma_f32_16x16x32_bf16 a[20:23], a[152:155], v[76:79], a[20:23]\n"
        "ds_read_b128 a[100:103], v7 offset:2048\n"
        "v_cvt_pk_bf16_f32 v90, v210, v214\n"
        "buffer_load_dwordx4 v5, s[36:39], s62 offen  lds\n"
        "v_mfma_f32_16x16x32_bf16 a[28:31], a[156:159], v[76:79], a[28:31]\n"
        "ds_read_b128 a[104:107], v7 offset:4096\n"
        "v_cndmask_b32_dpp v81, v89, v200, vcc row_ror:8 row_mask:0xf bank_mask:0xf\n"
        "v_mfma_f32_16x16x32_bf16 a[36:39], a[160:163], v[76:79], a[36:39]\n"
        "ds_read_b128 a[108:111], v7 offset:6144\n"
        "v_mov_b32_dpp v89, v200 row_ror:8 row_mask:0xf bank_mask:0x3\n"
        "s_add_u32 m0, s54, 40960\n"
        "v_mfma_f32_16x16x32_bf16 a[44:47], a[164:167], v[76:79], a[44:47]\n"
        "ds_read_b128 a[112:115], v7 offset:8192\n"
        "ds_read_b128 a[116:119], v7 offset:10240\n"
        "v_cvt_pk_bf16_f32 v216, v216, v220\n"
        "v_mfma_f32_16x16x32_bf16 a[52:55], a[168:171], v[76:79], a[52:55]\n"
        "ds_read_b128 a[120:123], v7 offset:12288\n"
        "buffer_load_dwordx4 v5, s[36:39], s63 offen  lds\n"
        "v_mfma_f32_16x16x32_bf16 a[60:63], a[172:175], v[76:79], a[60:63]\n"
        "v_cvt_pk_bf16_f32 v91, v218, v222\n"
        "ds_read_b128 a[124:127], v7 offset:14336\n"
        "v_cndmask_b32_dpp v82, v90, v208, vcc row_ror:8 row_mask:0xf bank_mask:0xf\n"
        "v_mfma_f32_16x16x32_bf16 a[68:71], a[176:179], v[76:79], a[68:71]\n"
        "ds_read_b128 a[128:131], v7 offset:16384\n"
        "s_add_u32 m0, s54, 45056\n"
        "v_mfma_f32_16x16x32_bf16 a[76:79], a[180:183], v[76:79], a[76:79]\n"
        "v_mov_b32_dpp v90, v208 row_ror:8 row_mask:0xf bank_mask:0x3\n"
        "ds_read_b128 a[132:135], v7 offset:18432\n"
        "v_cndmask_b32_dpp v83, v91, v216, vcc row_ror:8 row_mask:0xf bank_mask:0xf\n"
        "v_mfma_f32_16x16x32_bf16 a[84:87], a[184:187], v[76:79], a[84:87]\n"
        "ds_read_b128 a[136:139], v7 offset:20480\n"
        "buffer_load_dwordx4 v5, s[36:39], s64 offen  lds\n"
        "v_mov_b32_dpp v91, v216 row_ror:8 row_mask:0xf bank_mask:0x3\n"
        "v_mfma_f32_16x16x32_bf16 a[92:95], a[188:191], v[76:79], a[92:95]\n"
        "ds_read_b128 a[140:143], v7 offset:22528\n"
        "s_waitcnt lgkmcnt(6)\n"
        "v_mfma_f32_16x16x32_bf16 a[0:3], a[96:99], v[80:83], a[0:3]\n"
        "v_add_u32 v0, -73728, v0\n"
        "ds_read_b128 v[8:11], v6 offset:2560\n"
        "v_fma_mix_f32 v193, v193, v12, v14 op_sel_hi:[1,0,0]\n"
        "v_mfma_f32_16x16x32_bf16 a[8:11], a[100:103], v[80:83], a[8:11]\n"
        "ds_read_b128 a[144:147], v0 offset:0\n"
        "s_mul_i32 s33, s50, 9216\n"
        "v_mfma_f32_16x16x32_bf16 a[16:19], a[104:107], v[80:83], a[16:19]\n"
        "v_fma_mix_f32 v197, v197, v12, v14 op_sel_hi:[1,0,0]\n"
        "ds_read_b128 a[148:151], v0 offset:2048\n"
        "v_fma_mix_f32 v195, v195, v13, v15 op_sel_hi:[1,0,0]\n"
        "v_mfma_f32_16x16x32_bf16 a[24:27], a[108:111], v[80:83], a[24:27]\n"
        "ds_read_b128 a[152:155], v0 offset:4096\n"
        "ds_read_b128 a[156:159], v0 offset:6144\n"
        "v_mfma_f32_16x16x32_bf16 a[32:35], a[112:115], v[80:83], a[32:35]\n"
        "v_fma_mix_f32 v199, v199, v13, v15 op_sel_hi:[1,0,0]\n"
        "s_add_u32 s34, s33, 73728\n"
        "v_mfma_f32_16x16x32_bf16 a[40:43], a[116:119], v[80:83], a[40:43]\n"
        "ds_read_b128 a[160:163], v0 offset:8192\n"
        "v_fma_mix_f32 v201, v201, v12, v14 op_sel_hi:[1,0,0]\n"
        "s_waitcnt lgkmcnt(6)\n"
        "v_mfma_f32_16x16x32_bf16 a[48:51], a[120:123], v[80:83], a[48:51]\n"
        "ds_read_b128 a[164:167], v0 offset:10240\n"
        "v_fma_mix_f32 v205, v205, v12, v14 op_sel_hi:[1,0,0]\n"
        "ds_read_b128 a[168:171], v0 offset:12288\n"
        "v_mfma_f32_16x16x32_bf16 a[56:59], a[124:127], v[80:83], a[56:59]\n"
        "buffer_load_dwordx4 v[160:163], v1, s[28:31], s33 offen\n"
        "v_fma_mix_f32 v203, v203, v13, v15 op_sel_hi:[1,0,0]\n"
        "v_mfma_f32_16x16x32_bf16 a[64:67], a[128:131], v[80:83], a[64:67]\n"
        "ds_read_b128 a[172:175], v0 offset:14336\n"
        "ds_read_b128 a[176:179], v0 offset:16384\n"
        "v_mfma_f32_16x16x32_bf16 a[72:75], a[132:135], v[80:83], a[72:75]\n"
        "v_fma_mix_f32 v207, v207, v13, v15 op_sel_hi:[1,0,0]\n"
        "ds_read_b128 a[180:183], v0 offset:18432\n"
        "v_mfma_f32_16x16x32_bf16 a[80:83], a[136:139], v[80:83], a[80:83]\n"
        "v_cvt_pk_bf16_f32 v193, v193, v197\n"
        "buffer_load_dwordx4 v[164:167], v2, s[28:31], s33 offen\n"
        "ds_read_b128 a[184:187], v0 offset:20480\n"
        "v_mfma_f32_16x16x32_bf16 a[88:91], a[140:143], v[80:83], a[88:91]\n"
        "v_cvt_pk_bf16_f32 v92, v195, v199\n"
        "ds_read_b128 a[188:191], v0 offset:22528\n"
        "s_waitcnt lgkmcnt(6)\n"
        "v_mfma_f32_16x16x32_bf16 a[0:3], a[144:147], v[88:91], a[0:3]\n"
        "v_fma_mix_f32 v209, v209, v12, v14 op_sel_hi:[1,0,0]\n"
        "v_fma_mix_f32 v213, v213, v12, v14 op_sel_hi:[1,0,0]\n"
        "v_mfma_f32_16x16x32_bf16 a[8:11], a[148:151], v[88:91], a[8:11]\n"
        "buffer_load_dwordx4 v[168:171], v3, s[28:31], s33 offen\n"
        "v_fma_mix_f32 v211, v211, v13, v15 op_sel_hi:[1,0,0]\n"
        "v_fma_mix_f32 v215, v215, v13, v15 op_sel_hi:[1,0,0]\n"
        "v_mfma_f32_16x16x32_bf16 a[16:19], a[152:155], v[88:91], a[16:19]\n"
        "v_cvt_pk_bf16_f32 v201, v201, v205\n"
        "v_cvt_pk_bf16_f32 v93, v203, v207\n"
        "v_mfma_f32_16x16x32_bf16 a[24:27], a[156:159], v[88:91], a[24:27]\n"
        "buffer_load_dwordx4 v[172:175], v4, s[28:31], s33 offen\n"
        "v_cndmask_b32_dpp v84, v92, v193, vcc row_ror:8 row_mask:0xf bank_mask:0xf\n"
        "v_mfma_f32_16x16x32_bf16 a[32:35], a[160:163], v[88:91], a[32:35]\n"
        "v_mov_b32_dpp v92, v193 row_ror:8 row_mask:0xf bank_mask:0x3\n"
        "v_fma_mix_f32 v217, v217, v12, v14 op_sel_hi:[1,0,0]\n"
        "buffer_load_dwordx4 v[176:179], v1, s[28:31], s34 offen\n"
        "v_mfma_f32_16x16x32_bf16 a[40:43], a[164:167], v[88:91], a[40:43]\n"
        "v_fma_mix_f32 v221, v221, v12, v14 op_sel_hi:[1,0,0]\n"
        "v_fma_mix_f32 v219, v219, v13, v15 op_sel_hi:[1,0,0]\n"
        "s_waitcnt lgkmcnt(0)\n"
        "v_mfma_f32_16x16x32_bf16 a[48:51], a[168:171], v[88:91], a[48:51]\n"
        "v_fma_mix_f32 v223, v223, v13, v15 op_sel_hi:[1,0,0]\n"
        "v_cvt_pk_bf16_f32 v209, v209, v213\n"
        "v_mfma_f32_16x16x32_bf16 a[56:59], a[172:175], v[88:91], a[56:59]\n"
        "buffer_load_dwordx4 v[180:183], v2, s[28:31], s34 offen\n"
        "v_cvt_pk_bf16_f32 v94, v211, v215\n"
        "v_cndmask_b32_dpp v85, v93, v201, vcc row_ror:8 row_mask:0xf bank_mask:0xf\n"
        "v_mfma_f32_16x16x32_bf16 a[64:67], a[176:179], v[88:91], a[64:67]\n"
        "v_mov_b32_dpp v93, v201 row_ror:8 row_mask:0xf bank_mask:0x3\n"
        "v_cvt_pk_bf16_f32 v217, v217, v221\n"
        "v_mfma_f32_16x16x32_bf16 a[72:75], a[180:183], v[88:91], a[72:75]\n"
        "buffer_load_dwordx4 v[184:187], v3, s[28:31], s34 offen\n"
        "v_cvt_pk_bf16_f32 v95, v219, v223\n"
        "v_mfma_f32_16x16x32_bf16 a[80:83], a[184:187], v[88:91], a[80:83]\n"
        "v_cndmask_b32_dpp v86, v94, v209, vcc row_ror:8 row_mask:0xf bank_mask:0xf\n"
        "v_mov_b32_dpp v94, v209 row_ror:8 row_mask:0xf bank_mask:0x3\n"
        "buffer_load_dwordx4 v[188:191], v4, s[28:31], s34 offen\n"
        "v_mfma_f32_16x16x32_bf16 a[88:91], a[188:191], v[88:91], a[88:91]\n"
        "v_cndmask_b32_dpp v87, v95, v217, vcc row_ror:8 row_mask:0xf bank_mask:0xf\n"
        "v_mov_b32_dpp v95, v217 row_ror:8 row_mask:0xf bank_mask:0x3\n"
        "s_nop 0\n"
        "v_mfma_f32_16x16x32_bf16 a[4:7], a[96:99], v[84:87], a[4:7]\n"
        "ds_read_b128 v[12:15], v6 offset:2816\n"
        "s_sub_u32 s35, s50, s49\n"
        "v_mfma_f32_16x16x32_bf16 a[12:15], a[100:103], v[84:87], a[12:15]\n"
        "s_waitcnt vmcnt(18)\n"
        "v_fma_mix_f32 v224, v224, v8, v10 op_sel_hi:[1,0,0]\n"
        "v_fma_mix_f32 v228, v228, v8, v10 op_sel_hi:[1,0,0]\n"
        "v_fma_mix_f32 v226, v226, v9, v11 op_sel_hi:[1,0,0]\n"
        "v_mfma_f32_16x16x32_bf16 a[20:23], a[104:107], v[84:87], a[20:23]\n"
        "v_add_u32 v5, s35, v5\n"
        "v_fma_mix_f32 v230, v230, v9, v11 op_sel_hi:[1,0,0]\n"
        "v_mfma_f32_16x16x32_bf16 a[28:31], a[108:111], v[84:87], a[28:31]\n"
        "v_fma_mix_f32 v232, v232, v8, v10 op_sel_hi:[1,0,0]\n"
        "v_fma_mix_f32 v236, v236, v8, v10 op_sel_hi:[1,0,0]\n"
        "s_add_u32 m0, s54, 49152\n"
        "v_mfma_f32_16x16x32_bf16 a[36:39], a[112:115], v[84:87], a[36:39]\n"
        "v_fma_mix_f32 v234, v234, v9, v11 op_sel_hi:[1,0,0]\n"
        "v_fma_mix_f32 v238, v238, v9, v11 op_sel_hi:[1,0,0]\n"
        "v_mfma_f32_16x16x32_bf16 a[44:47], a[116:119], v[84:87], a[44:47]\n"
        "v_cvt_pk_bf16_f32 v224, v224, v228\n"
        "buffer_load_dwordx4 v5, s[36:39], s59 offen  lds\n"
        "v_cvt_pk_bf16_f32 v104, v226, v230\n"
        "v_mfma_f32_16x16x32_bf16 a[52:55], a[120:123], v[84:87], a[52:55]\n"
        "s_waitcnt vmcnt(15)\n"
        "v_fma_mix_f32 v240, v240, v8, v10 op_sel_hi:[1,0,0]\n"
        "s_add_u32 m0, s54, 53248\n"
        "v_mfma_f32_16x16x32_bf16 a[60:63], a[124:127], v[84:87], a[60:63]\n"
        "v_fma_mix_f32 v244, v244, v8, v10 op_sel_hi:[1,0,0]\n"
        "v_fma_mix_f32 v242, v242, v9, v11 op_sel_hi:[1,0,0]\n"
        "v_fma_mix_f32 v246, v246, v9, v11 op_sel_hi:[1,0,0]\n"
        "v_mfma_f32_16x16x32_bf16 a[68:71], a[128:131], v[84:87], a[68:71]\n"
        "buffer_load_dwordx4 v5, s[36:39], s60 offen  lds\n"
        "v_cvt_pk_bf16_f32 v232, v232, v236\n"
        "v_mfma_f32_16x16x32_bf16 a[76:79], a[132:135], v[84:87], a[76:79]\n"
        "v_cvt_pk_bf16_f32 v105, v234, v238\n"
        "v_cndmask_b32_dpp v96, v104, v224, vcc row_ror:8 row_mask:0xf bank_mask:0xf\n"
        "s_add_u32 m0, s54, 57344\n"
        "v_mfma_f32_16x16x32_bf16 a[84:87], a[136:139], v[84:87], a[84:87]\n"
        "v_mov_b32_dpp v104, v224 row_ror:8 row_mask:0xf bank_mask:0x3\n"
        "v_fma_mix_f32 v248, v248, v8, v10 op_sel_hi:[1,0,0]\n"
        "v_mfma_f32_16x16x32_bf16 a[92:95], a[140:143], v[84:87], a[92:95]\n"
        "v_fma_mix_f32 v252, v252, v8, v10 op_sel_hi:[1,0,0]\n"
        "buffer_load_dwordx4 v5, s[36:39], s61 offen  lds\n"
        "v_fma_mix_f32 v250, v250, v9, v11 op_sel_hi:[1,0,0]\n"
        "s_waitcnt vmcnt(11)\n"
        "s_barrier\n"
        "v_mfma_f32_16x16x32_bf16 a[4:7], a[144:147], v[92:95], a[4:7]\n"
        "v_add_u32 v7, 24576, v7\n"
        "v_fma_mix_f32 v254, v254, v9, v11 op_sel_hi:[1,0,0]\n"
        "s_add_u32 m0, s54, 61440\n"
        "v_mfma_f32_16x16x32_bf16 a[12:15], a[148:151], v[92:95], a[12:15]\n"
        "ds_read_b128 a[96:99], v7 offset:0\n"
        "v_cvt_pk_bf16_f32 v240, v240, v244\n"
        "v_mfma_f32_16x16x32_bf16 a[20:23], a[152:155], v[92:95], a[20:23]\n"
        "ds_read_b128 a[100:103], v7 offset:2048\n"
        "v_cvt_pk_bf16_f32 v106, v242, v246\n"
        "buffer_load_dwordx4 v5, s[36:39], s62 offen  lds\n"
        "v_mfma_f32_16x16x32_bf16 a[28:31], a[156:159], v[92:95], a[28:31]\n"
        "ds_read_b128 a[104:107], v7 offset:4096\n"
        "v_cndmask_b32_dpp v97, v105, v232, vcc row_ror:8 row_mask:0xf bank_mask:0xf\n"
        "v_mfma_f32_16x16x32_bf16 a[36:39], a[160:163], v[92:95], a[36:39]\n"
        "ds_read_b128 a[108:111], v7 offset:6144\n"
        "v_mov_b32_dpp v105, v232 row_ror:8 row_mask:0xf bank_mask:0x3\n"
        "s_add_u32 m0, s54, 65536\n"
        "v_mfma_f32_16x16x32_bf16 a[44:47], a[164:167], v[92:95], a[44:47]\n"
        "ds_read_b128 a[112:115], v7 offset:8192\n"
        "ds_read_b128 a[116:119], v7 offset:10240\n"
        "v_cvt_pk_bf16_f32 v248, v248, v252\n"
        "v_mfma_f32_16x16x32_bf16 a[52:55], a[168:171], v[92:95], a[52:55]\n"
        "ds_read_b128 a[120:123], v7 offset:12288\n"
        "buffer_load_dwordx4 v5, s[36:39], s63 offen  lds\n"
        "v_mfma_f32_16x16x32_bf16 a[60:63], a[172:175], v[92:95], a[60:63]\n"
        "v_cvt_pk_bf16_f32 v107, v250, v254\n"
        "ds_read_b128 a[124:127], v7 offset:14336\n"
        "v_cndmask_b32_dpp v98, v106, v240, vcc row_ror:8 row_mask:0xf bank_mask:0xf\n"
        "v_mfma_f32_16x16x32_bf16 a[68:71], a[176:179], v[92:95], a[68:71]\n"
        "ds_read_b128 a[128:131], v7 offset:16384\n"
        "s_add_u32 m0, s54, 69632\n"
        "v_mfma_f32_16x16x32_bf16 a[76:79], a[180:183], v[92:95], a[76:79]\n"
        "v_mov_b32_dpp v106, v240 row_ror:8 row_mask:0xf bank_mask:0x3\n"
        "ds_read_b128 a[132:135], v7 offset:18432\n"
        "v_cndmask_b32_dpp v99, v107, v248, vcc row_ror:8 row_mask:0xf bank_mask:0xf\n"
        "v_mfma_f32_16x16x32_bf16 a[84:87], a[184:187], v[92:95], a[84:87]\n"
        "ds_read_b128 a[136:139], v7 offset:20480\n"
        "buffer_load_dwordx4 v5, s[36:39], s64 offen  lds\n"
        "v_mov_b32_dpp v107, v248 row_ror:8 row_mask:0xf bank_mask:0x3\n"
        "v_mfma_f32_16x16x32_bf16 a[92:95], a[188:191], v[92:95], a[92:95]\n"
        "ds_read_b128 a[140:143], v7 offset:22528\n"
        "s_waitcnt lgkmcnt(6)\n"
        "v_mfma_f32_16x16x32_bf16 a[0:3], a[96:99], v[96:99], a[0:3]\n"
        "v_add_u32 v0, 24576, v0\n"
        "ds_read_b128 v[8:11], v6 offset:3072\n"
        "v_fma_mix_f32 v225, v225, v12, v14 op_sel_hi:[1,0,0]\n"
        "v_mfma_f32_16x16x32_bf16 a[8:11], a[100:103], v[96:99], a[8:11]\n"
        "ds_read_b128 a[144:147], v0 offset:0\n"
        "s_mul_i32 s33, s51, 9216\n"
        "v_mfma_f32_16x16x32_bf16 a[16:19], a[104:107], v[96:99], a[16:19]\n"
        "v_fma_mix_f32 v229, v229, v12, v14 op_sel_hi:[1,0,0]\n"
        "ds_read_b128 a[148:151], v0 offset:2048\n"
        "v_fma_mix_f32 v227, v227, v13, v15 op_sel_hi:[1,0,0]\n"
        "v_mfma_f32_16x16x32_bf16 a[24:27], a[108:111], v[96:99], a[24:27]\n"
        "ds_read_b128 a[152:155], v0 offset:4096\n"
        "ds_read_b128 a[156:159], v0 offset:6144\n"
        "v_mfma_f32_16x16x32_bf16 a[32:35], a[112:115], v[96:99], a[32:35]\n"
        "v_fma_mix_f32 v231, v231, v13, v15 op_sel_hi:[1,0,0]\n"
        "s_add_u32 s34, s33, 73728\n"
        "v_mfma_f32_16x16x32_bf16 a[40:43], a[116:119], v[96:99], a[40:43]\n"
        "ds_read_b128 a[160:163], v0 offset:8192\n"
        "v_fma_mix_f32 v233, v233, v12, v14 op_sel_hi:[1,0,0]\n"
        "s_waitcnt lgkmcnt(6)\n"
        "v_mfma_f32_16x16x32_bf16 a[48:51], a[120:123], v[96:99], a[48:51]\n"
        "ds_read_b128 a[164:167], v0 offset:10240\n"
        "v_fma_mix_f32 v237, v237, v12, v14 op_sel_hi:[1,0,0]\n"
        "ds_read_b128 a[168:171], v0 offset:12288\n"
        "v_mfma_f32_16x16x32_bf16 a[56:59], a[124:127], v[96:99], a[56:59]\n"
        "buffer_load_dwordx4 v[192:195], v1, s[28:31], s33 offen\n"
        "v_fma_mix_f32 v235, v235, v13, v15 op_sel_hi:[1,0,0]\n"
        "v_mfma_f32_16x16x32_bf16 a[64:67], a[128:131], v[96:99], a[64:67]\n"
        "ds_read_b128 a[172:175], v0 offset:14336\n"
        "ds_read_b128 a[176:179], v0 offset:16384\n"
        "v_mfma_f32_16x16x32_bf16 a[72:75], a[132:135], v[96:99], a[72:75]\n"
        "v_fma_mix_f32 v239, v239, v13, v15 op_sel_hi:[1,0,0]\n"
        "ds_read_b128 a[180:183], v0 offset:18432\n"
        "v_mfma_f32_16x16x32_bf16 a[80:83], a[136:139], v[96:99], a[80:83]\n"
        "v_cvt_pk_bf16_f32 v225, v225, v229\n"
        "buffer_load_dwordx4 v[196:199], v2, s[28:31], s33 offen\n"
        "ds_read_b128 a[184:187], v0 offset:20480\n"
        "v_mfma_f32_16x16x32_bf16 a[88:91], a[140:143], v[96:99], a[88:91]\n"
        "v_cvt_pk_bf16_f32 v108, v227, v231\n"
        "ds_read_b128 a[188:191], v0 offset:22528\n"
        "s_waitcnt lgkmcnt(6)\n"
        "v_mfma_f32_16x16x32_bf16 a[0:3], a[144:147], v[104:107], a[0:3]\n"
        "v_fma_mix_f32 v241, v241, v12, v14 op_sel_hi:[1,0,0]\n"
        "v_fma_mix_f32 v245, v245, v12, v14 op_sel_hi:[1,0,0]\n"
        "v_mfma_f32_16x16x32_bf16 a[8:11], a[148:151], v[104:107], a[8:11]\n"
        "buffer_load_dwordx4 v[200:203], v3, s[28:31], s33 offen\n"
        "v_fma_mix_f32 v243, v243, v13, v15 op_sel_hi:[1,0,0]\n"
        "v_fma_mix_f32 v247, v247, v13, v15 op_sel_hi:[1,0,0]\n"
        "v_mfma_f32_16x16x32_bf16 a[16:19], a[152:155], v[104:107], a[16:19]\n"
        "v_cvt_pk_bf16_f32 v233, v233, v237\n"
        "v_cvt_pk_bf16_f32 v109, v235, v239\n"
        "v_mfma_f32_16x16x32_bf16 a[24:27], a[156:159], v[104:107], a[24:27]\n"
        "buffer_load_dwordx4 v[204:207], v4, s[28:31], s33 offen\n"
        "v_cndmask_b32_dpp v100, v108, v225, vcc row_ror:8 row_mask:0xf bank_mask:0xf\n"
        "v_mfma_f32_16x16x32_bf16 a[32:35], a[160:163], v[104:107], a[32:35]\n"
        "v_mov_b32_dpp v108, v225 row_ror:8 row_mask:0xf bank_mask:0x3\n"
        "v_fma_mix_f32 v249, v249, v12, v14 op_sel_hi:[1,0,0]\n"
        "buffer_load_dwordx4 v[208:211], v1, s[28:31], s34 offen\n"
        "v_mfma_f32_16x16x32_bf16 a[40:43], a[164:167], v[104:107], a[40:43]\n"
        "v_fma_mix_f32 v253, v253, v12, v14 op_sel_hi:[1,0,0]\n"
        "v_fma_mix_f32 v251, v251, v13, v15 op_sel_hi:[1,0,0]\n"
        "s_waitcnt lgkmcnt(0)\n"
        "v_mfma_f32_16x16x32_bf16 a[48:51], a[168:171], v[104:107], a[48:51]\n"
        "v_fma_mix_f32 v255, v255, v13, v15 op_sel_hi:[1,0,0]\n"
        "v_cvt_pk_bf16_f32 v241, v241, v245\n"
        "v_mfma_f32_16x16x32_bf16 a[56:59], a[172:175], v[104:107], a[56:59]\n"
        "buffer_load_dwordx4 v[212:215], v2, s[28:31], s34 offen\n"
        "v_cvt_pk_bf16_f32 v110, v243, v247\n"
        "v_cndmask_b32_dpp v101, v109, v233, vcc row_ror:8 row_mask:0xf bank_mask:0xf\n"
        "v_mfma_f32_16x16x32_bf16 a[64:67], a[176:179], v[104:107], a[64:67]\n"
        "v_mov_b32_dpp v109, v233 row_ror:8 row_mask:0xf bank_mask:0x3\n"
        "v_cvt_pk_bf16_f32 v249, v249, v253\n"
        "v_mfma_f32_16x16x32_bf16 a[72:75], a[180:183], v[104:107], a[72:75]\n"
        "buffer_load_dwordx4 v[216:219], v3, s[28:31], s34 offen\n"
        "v_cvt_pk_bf16_f32 v111, v251, v255\n"
        "v_mfma_f32_16x16x32_bf16 a[80:83], a[184:187], v[104:107], a[80:83]\n"
        "v_cndmask_b32_dpp v102, v110, v241, vcc row_ror:8 row_mask:0xf bank_mask:0xf\n"
        "v_mov_b32_dpp v110, v241 row_ror:8 row_mask:0xf bank_mask:0x3\n"
        "buffer_load_dwordx4 v[220:223], v4, s[28:31], s34 offen\n"
        "v_mfma_f32_16x16x32_bf16 a[88:91], a[188:191], v[104:107], a[88:91]\n"
        "v_cndmask_b32_dpp v103, v111, v249, vcc row_ror:8 row_mask:0xf bank_mask:0xf\n"
        "v_mov_b32_dpp v111, v249 row_ror:8 row_mask:0xf bank_mask:0x3\n"
        "s_nop 0\n"
        "v_mfma_f32_16x16x32_bf16 a[4:7], a[96:99], v[100:103], a[4:7]\n"
        "ds_read_b128 v[12:15], v6 offset:3328\n"
        "s_sub_u32 s35, s51, s50\n"
        "v_mfma_f32_16x16x32_bf16 a[12:15], a[100:103], v[100:103], a[12:15]\n"
        "s_waitcnt vmcnt(18)\n"
        "v_fma_mix_f32 v160, v160, v8, v10 op_sel_hi:[1,0,0]\n"
        "v_fma_mix_f32 v164, v164, v8, v10 op_sel_hi:[1,0,0]\n"
        "v_fma_mix_f32 v162, v162, v9, v11 op_sel_hi:[1,0,0]\n"
        "v_mfma_f32_16x16x32_bf16 a[20:23], a[104:107], v[100:103], a[20:23]\n"
        "v_add_u32 v5, s35, v5\n"
        "v_fma_mix_f32 v166, v166, v9, v11 op_sel_hi:[1,0,0]\n"
        "v_mfma_f32_16x16x32_bf16 a[28:31], a[108:111], v[100:103], a[28:31]\n"
        "v_fma_mix_f32 v168, v168, v8, v10 op_sel_hi:[1,0,0]\n"
        "v_fma_mix_f32 v172, v172, v8, v10 op_sel_hi:[1,0,0]\n"
        "s_add_u32 m0, s54, 73728\n"
        "v_mfma_f32_16x16x32_bf16 a[36:39], a[112:115], v[100:103], a[36:39]\n"
        "v_fma_mix_f32 v170, v170, v9, v11 op_sel_hi:[1,0,0]\n"
        "v_fma_mix_f32 v174, v174, v9, v11 op_sel_hi:[1,0,0]\n"
        "v_mfma_f32_16x16x32_bf16 a[44:47], a[116:119], v[100:103], a[44:47]\n"
        "v_cvt_pk_bf16_f32 v160, v160, v164\n"
        "buffer_load_dwordx4 v5, s[36:39], s59 offen  lds\n"
        "v_cvt_pk_bf16_f32 v120, v162, v166\n"
        "v_mfma_f32_16x16x32_bf16 a[52:55], a[120:123], v[100:103], a[52:55]\n"
        "s_waitcnt vmcnt(15)\n"
        "v_fma_mix_f32 v176, v176, v8, v10 op_sel_hi:[1,0,0]\n"
        "s_add_u32 m0, s54, 77824\n"
        "v_mfma_f32_16x16x32_bf16 a[60:63], a[124:127], v[100:103], a[60:63]\n"
        "v_fma_mix_f32 v180, v180, v8, v10 op_sel_hi:[1,0,0]\n"
        "v_fma_mix_f32 v178, v178, v9, v11 op_sel_hi:[1,0,0]\n"
        "v_fma_mix_f32 v182, v182, v9, v11 op_sel_hi:[1,0,0]\n"
        "v_mfma_f32_16x16x32_bf16 a[68:71], a[128:131], v[100:103], a[68:71]\n"
        "buffer_load_dwordx4 v5, s[36:39], s60 offen  lds\n"
        "v_cvt_pk_bf16_f32 v168, v168, v172\n"
        "v_mfma_f32_16x16x32_bf16 a[76:79], a[132:135], v[100:103], a[76:79]\n"
        "v_cvt_pk_bf16_f32 v121, v170, v174\n"
        "v_cndmask_b32_dpp v112, v120, v160, vcc row_ror:8 row_mask:0xf bank_mask:0xf\n"
        "s_add_u32 m0, s54, 81920\n"
        "v_mfma_f32_16x16x32_bf16 a[84:87], a[136:139], v[100:103], a[84:87]\n"
        "v_mov_b32_dpp v120, v160 row_ror:8 row_mask:0xf bank_mask:0x3\n"
        "v_fma_mix_f32 v184, v184, v8, v10 op_sel_hi:[1,0,0]\n"
        "v_mfma_f32_16x16x32_bf16 a[92:95], a[140:143], v[100:103], a[92:95]\n"
        "v_fma_mix_f32 v188, v188, v8, v10 op_sel_hi:[1,0,0]\n"
        "buffer_load_dwordx4 v5, s[36:39], s61 offen  lds\n"
        "v_fma_mix_f32 v186, v186, v9, v11 op_sel_hi:[1,0,0]\n"
        "s_waitcnt vmcnt(11)\n"
        "s_barrier\n"
        "v_mfma_f32_16x16x32_bf16 a[4:7], a[144:147], v[108:111], a[4:7]\n"
        "v_add_u32 v7, 24576, v7\n"
        "v_fma_mix_f32 v190, v190, v9, v11 op_sel_hi:[1,0,0]\n"
        "s_add_u32 m0, s54, 86016\n"
        "v_mfma_f32_16x16x32_bf16 a[12:15], a[148:151], v[108:111], a[12:15]\n"
        "ds_read_b128 a[96:99], v7 offset:0\n"
        "v_cvt_pk_bf16_f32 v176, v176, v180\n"
        "v_mfma_f32_16x16x32_bf16 a[20:23], a[152:155], v[108:111], a[20:23]\n"
        "ds_read_b128 a[100:103], v7 offset:2048\n"
        "v_cvt_pk_bf16_f32 v122, v178, v182\n"
        "buffer_load_dwordx4 v5, s[36:39], s62 offen  lds\n"
        "v_mfma_f32_16x16x32_bf16 a[28:31], a[156:159], v[108:111], a[28:31]\n"
        "ds_read_b128 a[104:107], v7 offset:4096\n"
        "v_cndmask_b32_dpp v113, v121, v168, vcc row_ror:8 row_mask:0xf bank_mask:0xf\n"
        "v_mfma_f32_16x16x32_bf16 a[36:39], a[160:163], v[108:111], a[36:39]\n"
        "ds_read_b128 a[108:111], v7 offset:6144\n"
        "v_mov_b32_dpp v121, v168 row_ror:8 row_mask:0xf bank_mask:0x3\n"
        "s_add_u32 m0, s54, 90112\n"
        "v_mfma_f32_16x16x32_bf16 a[44:47], a[164:167], v[108:111], a[44:47]\n"
        "ds_read_b128 a[112:115], v7 offset:8192\n"
        "ds_read_b128 a[116:119], v7 offset:10240\n"
        "v_cvt_pk_bf16_f32 v184, v184, v188\n"
        "v_mfma_f32_16x16x32_bf16 a[52:55], a[168:171], v[108:111], a[52:55]\n"
        "ds_read_b128 a[120:123], v7 offset:12288\n"
        "buffer_load_dwordx4 v5, s[36:39], s63 offen  lds\n"
        "v_mfma_f32_16x16x32_bf16 a[60:63], a[172:175], v[108:111], a[60:63]\n"
        "v_cvt_pk_bf16_f32 v123, v186, v190\n"
        "ds_read_b128 a[124:127], v7 offset:14336\n"
        "v_cndmask_b32_dpp v114, v122, v176, vcc row_ror:8 row_mask:0xf bank_mask:0xf\n"
        "v_mfma_f32_16x16x32_bf16 a[68:71], a[176:179], v[108:111], a[68:71]\n"
        "ds_read_b128 a[128:131], v7 offset:16384\n"
        "s_add_u32 m0, s54, 94208\n"
        "v_mfma_f32_16x16x32_bf16 a[76:79], a[180:183], v[108:111], a[76:79]\n"
        "v_mov_b32_dpp v122, v176 row_ror:8 row_mask:0xf bank_mask:0x3\n"
        "ds_read_b128 a[132:135], v7 offset:18432\n"
        "v_cndmask_b32_dpp v115, v123, v184, vcc row_ror:8 row_mask:0xf bank_mask:0xf\n"
        "v_mfma_f32_16x16x32_bf16 a[84:87], a[184:187], v[108:111], a[84:87]\n"
        "ds_read_b128 a[136:139], v7 offset:20480\n"
        "buffer_load_dwordx4 v5, s[36:39], s64 offen  lds\n"
        "v_mov_b32_dpp v123, v184 row_ror:8 row_mask:0xf bank_mask:0x3\n"
        "v_mfma_f32_16x16x32_bf16 a[92:95], a[188:191], v[108:111], a[92:95]\n"
        "ds_read_b128 a[140:143], v7 offset:22528\n"
        "s_waitcnt lgkmcnt(6)\n"
        "v_mfma_f32_16x16x32_bf16 a[0:3], a[96:99], v[112:115], a[0:3]\n"
        "v_add_u32 v0, 24576, v0\n"
        "ds_read_b128 v[8:11], v6 offset:3584\n"
        "v_fma_mix_f32 v161, v161, v12, v14 op_sel_hi:[1,0,0]\n"
        "v_mfma_f32_16x16x32_bf16 a[8:11], a[100:103], v[112:115], a[8:11]\n"
        "ds_read_b128 a[144:147], v0 offset:0\n"
        "s_mul_i32 s33, s52, 9216\n"
        "v_mfma_f32_16x16x32_bf16 a[16:19], a[104:107], v[112:115], a[16:19]\n"
        "v_fma_mix_f32 v165, v165, v12, v14 op_sel_hi:[1,0,0]\n"
        "ds_read_b128 a[148:151], v0 offset:2048\n"
        "v_fma_mix_f32 v163, v163, v13, v15 op_sel_hi:[1,0,0]\n"
        "v_mfma_f32_16x16x32_bf16 a[24:27], a[108:111], v[112:115], a[24:27]\n"
        "ds_read_b128 a[152:155], v0 offset:4096\n"
        "ds_read_b128 a[156:159], v0 offset:6144\n"
        "v_mfma_f32_16x16x32_bf16 a[32:35], a[112:115], v[112:115], a[32:35]\n"
        "v_fma_mix_f32 v167, v167, v13, v15 op_sel_hi:[1,0,0]\n"
        "s_add_u32 s34, s33, 73728\n"
        "v_mfma_f32_16x16x32_bf16 a[40:43], a[116:119], v[112:115], a[40:43]\n"
        "ds_read_b128 a[160:163], v0 offset:8192\n"
        "v_fma_mix_f32 v169, v169, v12, v14 op_sel_hi:[1,0,0]\n"
        "s_waitcnt lgkmcnt(6)\n"
        "v_mfma_f32_16x16x32_bf16 a[48:51], a[120:123], v[112:115], a[48:51]\n"
        "ds_read_b128 a[164:167], v0 offset:10240\n"
        "v_fma_mix_f32 v173, v173, v12, v14 op_sel_hi:[1,0,0]\n"
        "ds_read_b128 a[168:171], v0 offset:12288\n"
        "v_mfma_f32_16x16x32_bf16 a[56:59], a[124:127], v[112:115], a[56:59]\n"
        "buffer_load_dwordx4 v[224:227], v1, s[28:31], s33 offen\n"
        "v_fma_mix_f32 v171, v171, v13, v15 op_sel_hi:[1,0,0]\n"
        "v_mfma_f32_16x16x32_bf16 a[64:67], a[128:131], v[112:115], a[64:67]\n"
        "ds_read_b128 a[172:175], v0 offset:14336\n"
        "ds_read_b128 a[176:179], v0 offset:16384\n"
        "v_mfma_f32_16x16x32_bf16 a[72:75], a[132:135], v[112:115], a[72:75]\n"
        "v_fma_mix_f32 v175, v175, v13, v15 op_sel_hi:[1,0,0]\n"
        "ds_read_b128 a[180:183], v0 offset:18432\n"
        "v_mfma_f32_16x16x32_bf16 a[80:83], a[136:139], v[112:115], a[80:83]\n"
        "v_cvt_pk_bf16_f32 v161, v161, v165\n"
        "buffer_load_dwordx4 v[228:231], v2, s[28:31], s33 offen\n"
        "ds_read_b128 a[184:187], v0 offset:20480\n"
        "v_mfma_f32_16x16x32_bf16 a[88:91], a[140:143], v[112:115], a[88:91]\n"
        "v_cvt_pk_bf16_f32 v124, v163, v167\n"
        "ds_read_b128 a[188:191], v0 offset:22528\n"
        "s_waitcnt lgkmcnt(6)\n"
        "v_mfma_f32_16x16x32_bf16 a[0:3], a[144:147], v[120:123], a[0:3]\n"
        "v_fma_mix_f32 v177, v177, v12, v14 op_sel_hi:[1,0,0]\n"
        "v_fma_mix_f32 v181, v181, v12, v14 op_sel_hi:[1,0,0]\n"
        "v_mfma_f32_16x16x32_bf16 a[8:11], a[148:151], v[120:123], a[8:11]\n"
        "buffer_load_dwordx4 v[232:235], v3, s[28:31], s33 offen\n"
        "v_fma_mix_f32 v179, v179, v13, v15 op_sel_hi:[1,0,0]\n"
        "v_fma_mix_f32 v183, v183, v13, v15 op_sel_hi:[1,0,0]\n"
        "v_mfma_f32_16x16x32_bf16 a[16:19], a[152:155], v[120:123], a[16:19]\n"
        "v_cvt_pk_bf16_f32 v169, v169, v173\n"
        "v_cvt_pk_bf16_f32 v125, v171, v175\n"
        "v_mfma_f32_16x16x32_bf16 a[24:27], a[156:159], v[120:123], a[24:27]\n"
        "buffer_load_dwordx4 v[236:239], v4, s[28:31], s33 offen\n"
        "v_cndmask_b32_dpp v116, v124, v161, vcc row_ror:8 row_mask:0xf bank_mask:0xf\n"
        "v_mfma_f32_16x16x32_bf16 a[32:35], a[160:163], v[120:123], a[32:35]\n"
        "v_mov_b32_dpp v124, v161 row_ror:8 row_mask:0xf bank_mask:0x3\n"
        "v_fma_mix_f32 v185, v185, v12, v14 op_sel_hi:[1,0,0]\n"
        "buffer_load_dwordx4 v[240:243], v1, s[28:31], s34 offen\n"
        "v_mfma_f32_16x16x32_bf16 a[40:43], a[164:167], v[120:123], a[40:43]\n"
        "v_fma_mix_f32 v189, v189, v12, v14 op_sel_hi:[1,0,0]\n"
        "v_fma_mix_f32 v187, v187, v13, v15 op_sel_hi:[1,0,0]\n"
        "s_waitcnt lgkmcnt(0)\n"
        "v_mfma_f32_16x16x32_bf16 a[48:51], a[168:171], v[120:123], a[48:51]\n"
        "v_fma_mix_f32 v191, v191, v13, v15 op_sel_hi:[1,0,0]\n"
        "v_cvt_pk_bf16_f32 v177, v177, v181\n"
        "v_mfma_f32_16x16x32_bf16 a[56:59], a[172:175], v[120:123], a[56:59]\n"
        "buffer_load_dwordx4 v[244:247], v2, s[28:31], s34 offen\n"
        "v_cvt_pk_bf16_f32 v126, v179, v183\n"
        "v_cndmask_b32_dpp v117, v125, v169, vcc row_ror:8 row_mask:0xf bank_mask:0xf\n"
        "v_mfma_f32_16x16x32_bf16 a[64:67], a[176:179], v[120:123], a[64:67]\n"
        "v_mov_b32_dpp v125, v169 row_ror:8 row_mask:0xf bank_mask:0x3\n"
        "v_cvt_pk_bf16_f32 v185, v185, v189\n"
        "v_mfma_f32_16x16x32_bf16 a[72:75], a[180:183], v[120:123], a[72:75]\n"
        "buffer_load_dwordx4 v[248:251], v3, s[28:31], s34 offen\n"
        "v_cvt_pk_bf16_f32 v127, v187, v191\n"
        "v_mfma_f32_16x16x32_bf16 a[80:83], a[184:187], v[120:123], a[80:83]\n"
        "v_cndmask_b32_dpp v118, v126, v177, vcc row_ror:8 row_mask:0xf bank_mask:0xf\n"
        "v_mov_b32_dpp v126, v177 row_ror:8 row_mask:0xf bank_mask:0x3\n"
        "buffer_load_dwordx4 v[252:255], v4, s[28:31], s34 offen\n"
        "v_mfma_f32_16x16x32_bf16 a[88:91], a[188:191], v[120:123], a[88:91]\n"
        "v_cndmask_b32_dpp v119, v127, v185, vcc row_ror:8 row_mask:0xf bank_mask:0xf\n"
        "v_mov_b32_dpp v127, v185 row_ror:8 row_mask:0xf bank_mask:0x3\n"
        "s_nop 0\n"
        "v_mfma_f32_16x16x32_bf16 a[4:7], a[96:99], v[116:119], a[4:7]\n"
        "ds_read_b128 v[12:15], v6 offset:3840\n"
        "s_sub_u32 s35, s52, s51\n"
        "v_mfma_f32_16x16x32_bf16 a[12:15], a[100:103], v[116:119], a[12:15]\n"
        "s_waitcnt vmcnt(18)\n"
        "v_fma_mix_f32 v192, v192, v8, v10 op_sel_hi:[1,0,0]\n"
        "v_fma_mix_f32 v196, v196, v8, v10 op_sel_hi:[1,0,0]\n"
        "v_fma_mix_f32 v194, v194, v9, v11 op_sel_hi:[1,0,0]\n"
        "v_mfma_f32_16x16x32_bf16 a[20:23], a[104:107], v[116:119], a[20:23]\n"
        "v_add_u32 v5, s35, v5\n"
        "v_fma_mix_f32 v198, v198, v9, v11 op_sel_hi:[1,0,0]\n"
        "v_mfma_f32_16x16x32_bf16 a[28:31], a[108:111], v[116:119], a[28:31]\n"
        "v_fma_mix_f32 v200, v200, v8, v10 op_sel_hi:[1,0,0]\n"
        "v_fma_mix_f32 v204, v204, v8, v10 op_sel_hi:[1,0,0]\n"
        "s_add_u32 m0, s54, 0\n"
        "v_mfma_f32_16x16x32_bf16 a[36:39], a[112:115], v[116:119], a[36:39]\n"
        "v_fma_mix_f32 v202, v202, v9, v11 op_sel_hi:[1,0,0]\n"
        "v_fma_mix_f32 v206, v206, v9, v11 op_sel_hi:[1,0,0]\n"
        "v_mfma_f32_16x16x32_bf16 a[44:47], a[116:119], v[116:119], a[44:47]\n"
        "v_cvt_pk_bf16_f32 v192, v192, v196\n"
        "buffer_load_dwordx4 v5, s[36:39], s59 offen  lds\n"
        "v_cvt_pk_bf16_f32 v136, v194, v198\n"
        "v_mfma_f32_16x16x32_bf16 a[52:55], a[120:123], v[116:119], a[52:55]\n"
        "s_waitcnt vmcnt(15)\n"
        "v_fma_mix_f32 v208, v208, v8, v10 op_sel_hi:[1,0,0]\n"
        "s_add_u32 m0, s54, 4096\n"
        "v_mfma_f32_16x16x32_bf16 a[60:63], a[124:127], v[116:119], a[60:63]\n"
        "v_fma_mix_f32 v212, v212, v8, v10 op_sel_hi:[1,0,0]\n"
        "v_fma_mix_f32 v210, v210, v9, v11 op_sel_hi:[1,0,0]\n"
        "v_fma_mix_f32 v214, v214, v9, v11 op_sel_hi:[1,0,0]\n"
        "v_mfma_f32_16x16x32_bf16 a[68:71], a[128:131], v[116:119], a[68:71]\n"
        "buffer_load_dwordx4 v5, s[36:39], s60 offen  lds\n"
        "v_cvt_pk_bf16_f32 v200, v200, v204\n"
        "v_mfma_f32_16x16x32_bf16 a[76:79], a[132:135], v[116:119], a[76:79]\n"
        "v_cvt_pk_bf16_f32 v137, v202, v206\n"
        "v_cndmask_b32_dpp v128, v136, v192, vcc row_ror:8 row_mask:0xf bank_mask:0xf\n"
        "s_add_u32 m0, s54, 8192\n"
        "v_mfma_f32_16x16x32_bf16 a[84:87], a[136:139], v[116:119], a[84:87]\n"
        "v_mov_b32_dpp v136, v192 row_ror:8 row_mask:0xf bank_mask:0x3\n"
        "v_fma_mix_f32 v216, v216, v8, v10 op_sel_hi:[1,0,0]\n"
        "v_mfma_f32_16x16x32_bf16 a[92:95], a[140:143], v[116:119], a[92:95]\n"
        "v_fma_mix_f32 v220, v220, v8, v10 op_sel_hi:[1,0,0]\n"
        "buffer_load_dwordx4 v5, s[36:39], s61 offen  lds\n"
        "v_fma_mix_f32 v218, v218, v9, v11 op_sel_hi:[1,0,0]\n"
        "s_waitcnt vmcnt(11)\n"
        "s_barrier\n"
        "v_mfma_f32_16x16x32_bf16 a[4:7], a[144:147], v[124:127], a[4:7]\n"
        "v_add_u32 v7, 24576, v7\n"
        "v_fma_mix_f32 v222, v222, v9, v11 op_sel_hi:[1,0,0]\n"
        "s_add_u32 m0, s54, 12288\n"
        "v_mfma_f32_16x16x32_bf16 a[12:15], a[148:151], v[124:127], a[12:15]\n"
        "ds_read_b128 a[96:99], v7 offset:0\n"
        "v_cvt_pk_bf16_f32 v208, v208, v212\n"
        "v_mfma_f32_16x16x32_bf16 a[20:23], a[152:155], v[124:127], a[20:23]\n"
        "ds_read_b128 a[100:103], v7 offset:2048\n"
        "v_cvt_pk_bf16_f32 v138, v210, v214\n"
        "buffer_load_dwordx4 v5, s[36:39], s62 offen  lds\n"
        "v_mfma_f32_16x16x32_bf16 a[28:31], a[156:159], v[124:127], a[28:31]\n"
        "ds_read_b128 a[104:107], v7 offset:4096\n"
        "v_cndmask_b32_dpp v129, v137, v200, vcc row_ror:8 row_mask:0xf bank_mask:0xf\n"
        "v_mfma_f32_16x16x32_bf16 a[36:39], a[160:163], v[124:127], a[36:39]\n"
        "ds_read_b128 a[108:111], v7 offset:6144\n"
        "v_mov_b32_dpp v137, v200 row_ror:8 row_mask:0xf bank_mask:0x3\n"
        "s_add_u32 m0, s54, 16384\n"
        "v_mfma_f32_16x16x32_bf16 a[44:47], a[164:167], v[124:127], a[44:47]\n"
        "ds_read_b128 a[112:115], v7 offset:8192\n"
        "ds_read_b128 a[116:119], v7 offset:10240\n"
        "v_cvt_pk_bf16_f32 v216, v216, v220\n"
        "v_mfma_f32_16x16x32_bf16 a[52:55], a[168:171], v[124:127], a[52:55]\n"
        "ds_read_b128 a[120:123], v7 offset:12288\n"
        "buffer_load_dwordx4 v5, s[36:39], s63 offen  lds\n"
        "v_mfma_f32_16x16x32_bf16 a[60:63], a[172:175], v[124:127], a[60:63]\n"
        "v_cvt_pk_bf16_f32 v139, v218, v222\n"
        "ds_read_b128 a[124:127], v7 offset:14336\n"
        "v_cndmask_b32_dpp v130, v138, v208, vcc row_ror:8 row_mask:0xf bank_mask:0xf\n"
        "v_mfma_f32_16x16x32_bf16 a[68:71], a[176:179], v[124:127], a[68:71]\n"
        "ds_read_b128 a[128:131], v7 offset:16384\n"
        "s_add_u32 m0, s54, 20480\n"
        "v_mfma_f32_16x16x32_bf16 a[76:79], a[180:183], v[124:127], a[76:79]\n"
        "v_mov_b32_dpp v138, v208 row_ror:8 row_mask:0xf bank_mask:0x3\n"
        "ds_read_b128 a[132:135], v7 offset:18432\n"
        "v_cndmask_b32_dpp v131, v139, v216, vcc row_ror:8 row_mask:0xf bank_mask:0xf\n"
        "v_mfma_f32_16x16x32_bf16 a[84:87], a[184:187], v[124:127], a[84:87]\n"
        "ds_read_b128 a[136:139], v7 offset:20480\n"
        "buffer_load_dwordx4 v5, s[36:39], s64 offen  lds\n"
        "v_mov_b32_dpp v139, v216 row_ror:8 row_mask:0xf bank_mask:0x3\n"
        "v_mfma_f32_16x16x32_bf16 a[92:95], a[188:191], v[124:127], a[92:95]\n"
        "ds_read_b128 a[140:143], v7 offset:22528\n"
        "s_waitcnt lgkmcnt(6)\n"
        "v_mfma_f32_16x16x32_bf16 a[0:3], a[96:99], v[128:131], a[0:3]\n"
        "v_add_u32 v0, 24576, v0\n"
        "v_subrev_u32 v5, s52, v5\n"
        "ds_read_b128 v[8:11], v6 offset:4096\n"
        "v_fma_mix_f32 v193, v193, v12, v14 op_sel_hi:[1,0,0]\n"
        "v_mfma_f32_16x16x32_bf16 a[8:11], a[100:103], v[128:131], a[8:11]\n"
        "v_add_u32 v176, s44, v5\n"
        "ds_read_b128 a[144:147], v0 offset:0\n"
        "v_fma_mix_f32 v197, v197, v12, v14 op_sel_hi:[1,0,0]\n"
        "v_mfma_f32_16x16x32_bf16 a[16:19], a[104:107], v[128:131], a[16:19]\n"
        "v_add_u32 v177, s45, v5\n"
        "ds_read_b128 a[148:151], v0 offset:2048\n"
        "v_fma_mix_f32 v195, v195, v13, v15 op_sel_hi:[1,0,0]\n"
        "v_mfma_f32_16x16x32_bf16 a[24:27], a[108:111], v[128:131], a[24:27]\n"
        "v_add_u32 v178, s46, v5\n"
        "ds_read_b128 a[152:155], v0 offset:4096\n"
        "v_add_u32 v179, s47, v5\n"
        "v_mfma_f32_16x16x32_bf16 a[32:35], a[112:115], v[128:131], a[32:35]\n"
        "ds_read_b128 a[156:159], v0 offset:6144\n"
        "v_fma_mix_f32 v199, v199, v13, v15 op_sel_hi:[1,0,0]\n"
        "v_add_u32 v180, s48, v5\n"
        "v_mfma_f32_16x16x32_bf16 a[40:43], a[116:119], v[128:131], a[40:43]\n"
        "ds_read_b128 a[160:163], v0 offset:8192\n"
        "v_fma_mix_f32 v201, v201, v12, v14 op_sel_hi:[1,0,0]\n"
        "v_add_u32 v181, s49, v5\n"
        "s_waitcnt lgkmcnt(6)\n"
        "v_mfma_f32_16x16x32_bf16 a[48:51], a[120:123], v[128:131], a[48:51]\n"
        "ds_read_b128 a[164:167], v0 offset:10240\n"
        "v_add_u32 v182, s50, v5\n"
        "v_fma_mix_f32 v205, v205, v12, v14 op_sel_hi:[1,0,0]\n"
        "ds_read_b128 a[168:171], v0 offset:12288\n"
        "v_mfma_f32_16x16x32_bf16 a[56:59], a[124:127], v[128:131], a[56:59]\n"
        "v_add_u32 v183, s51, v5\n"
        "v_fma_mix_f32 v203, v203, v13, v15 op_sel_hi:[1,0,0]\n"
        "ds_read_b128 a[172:175], v0 offset:14336\n"
        "v_mfma_f32_16x16x32_bf16 a[64:67], a[128:131], v[128:131], a[64:67]\n"
        "v_add_u32 v184, s52, v5\n"
        "ds_read_b128 a[176:179], v0 offset:16384\n"
        "s_add_u32 m0, s55, 98304\n"
        "v_mfma_f32_16x16x32_bf16 a[72:75], a[132:135], v[128:131], a[72:75]\n"
        "v_fma_mix_f32 v207, v207, v13, v15 op_sel_hi:[1,0,0]\n"
        "ds_read_b128 a[180:183], v0 offset:18432\n"
        "buffer_load_dwordx4 v176, s[36:39], s65 offen  lds\n"
        "v_mfma_f32_16x16x32_bf16 a[80:83], a[136:139], v[128:131], a[80:83]\n"
        "v_cvt_pk_bf16_f32 v193, v193, v197\n"
        "ds_read_b128 a[184:187], v0 offset:20480\n"
        "s_add_u32 m0, s55, 100352\n"
        "v_mfma_f32_16x16x32_bf16 a[88:91], a[140:143], v[128:131], a[88:91]\n"
        "v_cvt_pk_bf16_f32 v140, v195, v199\n"
        "buffer_load_dwordx4 v177, s[36:39], s65 offen  lds\n"
        "ds_read_b128 a[188:191], v0 offset:22528\n"
        "s_waitcnt lgkmcnt(6)\n"
        "v_mfma_f32_16x16x32_bf16 a[0:3], a[144:147], v[136:139], a[0:3]\n"
        "v_fma_mix_f32 v209, v209, v12, v14 op_sel_hi:[1,0,0]\n"
        "s_add_u32 m0, s55, 102400\n"
        "v_fma_mix_f32 v213, v213, v12, v14 op_sel_hi:[1,0,0]\n"
        "v_mfma_f32_16x16x32_bf16 a[8:11], a[148:151], v[136:139], a[8:11]\n"
        "buffer_load_dwordx4 v178, s[36:39], s65 offen  lds\n"
        "v_fma_mix_f32 v211, v211, v13, v15 op_sel_hi:[1,0,0]\n"
        "v_fma_mix_f32 v215, v215, v13, v15 op_sel_hi:[1,0,0]\n"
        "v_mfma_f32_16x16x32_bf16 a[16:19], a[152:155], v[136:139], a[16:19]\n"
        "s_add_u32 m0, s55, 104448\n"
        "v_cvt_pk_bf16_f32 v201, v201, v205\n"
        "v_cvt_pk_bf16_f32 v141, v203, v207\n"
        "v_mfma_f32_16x16x32_bf16 a[24:27], a[156:159], v[136:139], a[24:27]\n"
        "buffer_load_dwordx4 v179, s[36:39], s65 offen  lds\n"
        "v_cndmask_b32_dpp v132, v140, v193, vcc row_ror:8 row_mask:0xf bank_mask:0xf\n"
        "s_add_u32 m0, s55, 106496\n"
        "v_mfma_f32_16x16x32_bf16 a[32:35], a[160:163], v[136:139], a[32:35]\n"
        "v_mov_b32_dpp v140, v193 row_ror:8 row_mask:0xf bank_mask:0x3\n"
        "v_fma_mix_f32 v217, v217, v12, v14 op_sel_hi:[1,0,0]\n"
        "buffer_load_dwordx4 v180, s[36:39], s65 offen  lds\n"
        "v_mfma_f32_16x16x32_bf16 a[40:43], a[164:167], v[136:139], a[40:43]\n"
        "v_fma_mix_f32 v221, v221, v12, v14 op_sel_hi:[1,0,0]\n"
        "s_add_u32 m0, s55, 108544\n"
        "v_fma_mix_f32 v219, v219, v13, v15 op_sel_hi:[1,0,0]\n"
        "s_waitcnt lgkmcnt(0)\n"
        "v_mfma_f32_16x16x32_bf16 a[48:51], a[168:171], v[136:139], a[48:51]\n"
        "v_fma_mix_f32 v223, v223, v13, v15 op_sel_hi:[1,0,0]\n"
        "buffer_load_dwordx4 v181, s[36:39], s65 offen  lds\n"
        "v_cvt_pk_bf16_f32 v209, v209, v213\n"
        "v_mfma_f32_16x16x32_bf16 a[56:59], a[172:175], v[136:139], a[56:59]\n"
        "s_add_u32 m0, s55, 110592\n"
        "v_cvt_pk_bf16_f32 v142, v211, v215\n"
        "v_cndmask_b32_dpp v133, v141, v201, vcc row_ror:8 row_mask:0xf bank_mask:0xf\n"
        "v_mfma_f32_16x16x32_bf16 a[64:67], a[176:179], v[136:139], a[64:67]\n"
        "buffer_load_dwordx4 v182, s[36:39], s65 offen  lds\n"
        "v_mov_b32_dpp v141, v201 row_ror:8 row_mask:0xf bank_mask:0x3\n"
        "v_cvt_pk_bf16_f32 v217, v217, v221\n"
        "v_mfma_f32_16x16x32_bf16 a[72:75], a[180:183], v[136:139], a[72:75]\n"
        "s_add_u32 m0, s55, 112640\n"
        "v_cvt_pk_bf16_f32 v143, v219, v223\n"
        "buffer_load_dwordx4 v183, s[36:39], s65 offen  lds\n"
        "v_mfma_f32_16x16x32_bf16 a[80:83], a[184:187], v[136:139], a[80:83]\n"
        "v_cndmask_b32_dpp v134, v142, v209, vcc row_ror:8 row_mask:0xf bank_mask:0xf\n"
        "v_mov_b32_dpp v142, v209 row_ror:8 row_mask:0xf bank_mask:0x3\n"
        "s_add_u32 m0, s55, 114688\n"
        "v_mfma_f32_16x16x32_bf16 a[88:91], a[188:191], v[136:139], a[88:91]\n"
        "v_cndmask_b32_dpp v135, v143, v217, vcc row_ror:8 row_mask:0xf bank_mask:0xf\n"
        "buffer_load_dwordx4 v184, s[36:39], s65 offen  lds\n"
        "v_mov_b32_dpp v143, v217 row_ror:8 row_mask:0xf bank_mask:0x3\n"
        "v_mfma_f32_16x16x32_bf16 a[4:7], a[96:99], v[132:135], a[4:7]\n"
        "ds_read_b128 v[12:15], v6 offset:4352\n"
        "s_waitcnt vmcnt(19)\n"
        "v_fma_mix_f32 v224, v224, v8, v10 op_sel_hi:[1,0,0]\n"
        "v_mfma_f32_16x16x32_bf16 a[12:15], a[100:103], v[132:135], a[12:15]\n"
        "s_add_u32 m0, s55, 24576\n"
        "v_fma_mix_f32 v228, v228, v8, v10 op_sel_hi:[1,0,0]\n"
        "v_fma_mix_f32 v226, v226, v9, v11 op_sel_hi:[1,0,0]\n"
        "v_mfma_f32_16x16x32_bf16 a[20:23], a[104:107], v[132:135], a[20:23]\n"
        "v_fma_mix_f32 v230, v230, v9, v11 op_sel_hi:[1,0,0]\n"
        "v_fma_mix_f32 v232, v232, v8, v10 op_sel_hi:[1,0,0]\n"
        "v_mfma_f32_16x16x32_bf16 a[28:31], a[108:111], v[132:135], a[28:31]\n"
        "buffer_load_dwordx4 v176, s[36:39], s66 offen  lds\n"
        "v_fma_mix_f32 v236, v236, v8, v10 op_sel_hi:[1,0,0]\n"
        "v_mfma_f32_16x16x32_bf16 a[36:39], a[112:115], v[132:135], a[36:39]\n"
        "v_fma_mix_f32 v234, v234, v9, v11 op_sel_hi:[1,0,0]\n"
        "v_fma_mix_f32 v238, v238, v9, v11 op_sel_hi:[1,0,0]\n"
        "s_add_u32 m0, s55, 26624\n"
        "v_mfma_f32_16x16x32_bf16 a[44:47], a[116:119], v[132:135], a[44:47]\n"
        "v_cvt_pk_bf16_f32 v224, v224, v228\n"
        "v_cvt_pk_bf16_f32 v152, v226, v230\n"
        "v_mfma_f32_16x16x32_bf16 a[52:55], a[120:123], v[132:135], a[52:55]\n"
        "s_waitcnt vmcnt(16)\n"
        "v_fma_mix_f32 v240, v240, v8, v10 op_sel_hi:[1,0,0]\n"
        "v_fma_mix_f32 v244, v244, v8, v10 op_sel_hi:[1,0,0]\n"
        "v_mfma_f32_16x16x32_bf16 a[60:63], a[124:127], v[132:135], a[60:63]\n"
        "buffer_load_dwordx4 v177, s[36:39], s66 offen  lds\n"
        "v_fma_mix_f32 v242, v242, v9, v11 op_sel_hi:[1,0,0]\n"
        "v_fma_mix_f32 v246, v246, v9, v11 op_sel_hi:[1,0,0]\n"
        "v_mfma_f32_16x16x32_bf16 a[68:71], a[128:131], v[132:135], a[68:71]\n"
        "v_cvt_pk_bf16_f32 v232, v232, v236\n"
        "v_cvt_pk_bf16_f32 v153, v234, v238\n"
        "v_mfma_f32_16x16x32_bf16 a[76:79], a[132:135], v[132:135], a[76:79]\n"
        "s_add_u32 m0, s55, 28672\n"
        "v_cndmask_b32_dpp v144, v152, v224, vcc row_ror:8 row_mask:0xf bank_mask:0xf\n"
        "v_mfma_f32_16x16x32_bf16 a[84:87], a[136:139], v[132:135], a[84:87]\n"
        "v_mov_b32_dpp v152, v224 row_ror:8 row_mask:0xf bank_mask:0x3\n"
        "v_fma_mix_f32 v248, v248, v8, v10 op_sel_hi:[1,0,0]\n"
        "buffer_load_dwordx4 v178, s[36:39], s66 offen  lds\n"
        "v_mfma_f32_16x16x32_bf16 a[92:95], a[140:143], v[132:135], a[92:95]\n"
        "v_fma_mix_f32 v252, v252, v8, v10 op_sel_hi:[1,0,0]\n"
        "v_fma_mix_f32 v250, v250, v9, v11 op_sel_hi:[1,0,0]\n"
        "s_waitcnt vmcnt(12)\n"
        "s_barrier\n"
        "v_mfma_f32_16x16x32_bf16 a[4:7], a[144:147], v[140:143], a[4:7]\n"
        "v_add_u32 v7, -73728, v7\n"
        "v_fma_mix_f32 v254, v254, v9, v11 op_sel_hi:[1,0,0]\n"
        "v_mfma_f32_16x16x32_bf16 a[12:15], a[148:151], v[140:143], a[12:15]\n"
        "ds_read_b128 a[96:99], v7 offset:0\n"
        "s_add_u32 m0, s55, 30720\n"
        "v_cvt_pk_bf16_f32 v240, v240, v244\n"
        "v_mfma_f32_16x16x32_bf16 a[20:23], a[152:155], v[140:143], a[20:23]\n"
        "ds_read_b128 a[100:103], v7 offset:2048\n"
        "v_cvt_pk_bf16_f32 v154, v242, v246\n"
        "v_mfma_f32_16x16x32_bf16 a[28:31], a[156:159], v[140:143], a[28:31]\n"
        "ds_read_b128 a[104:107], v7 offset:4096\n"
        "v_cndmask_b32_dpp v145, v153, v232, vcc row_ror:8 row_mask:0xf bank_mask:0xf\n"
        "ds_read_b128 a[108:111], v7 offset:6144\n"
        "v_mfma_f32_16x16x32_bf16 a[36:39], a[160:163], v[140:143], a[36:39]\n"
        "buffer_load_dwordx4 v179, s[36:39], s66 offen  lds\n"
        "v_mov_b32_dpp v153, v232 row_ror:8 row_mask:0xf bank_mask:0x3\n"
        "v_mfma_f32_16x16x32_bf16 a[44:47], a[164:167], v[140:143], a[44:47]\n"
        "ds_read_b128 a[112:115], v7 offset:8192\n"
        "ds_read_b128 a[116:119], v7 offset:10240\n"
        "v_cvt_pk_bf16_f32 v248, v248, v252\n"
        "v_mfma_f32_16x16x32_bf16 a[52:55], a[168:171], v[140:143], a[52:55]\n"
        "ds_read_b128 a[120:123], v7 offset:12288\n"
        "v_cvt_pk_bf16_f32 v155, v250, v254\n"
        "v_mfma_f32_16x16x32_bf16 a[60:63], a[172:175], v[140:143], a[60:63]\n"
        "s_add_u32 m0, s55, 32768\n"
        "ds_read_b128 a[124:127], v7 offset:14336\n"
        "v_mfma_f32_16x16x32_bf16 a[68:71], a[176:179], v[140:143], a[68:71]\n"
        "v_cndmask_b32_dpp v146, v154, v240, vcc row_ror:8 row_mask:0xf bank_mask:0xf\n"
        "ds_read_b128 a[128:131], v7 offset:16384\n"
        "v_mov_b32_dpp v154, v240 row_ror:8 row_mask:0xf bank_mask:0x3\n"
        "v_mfma_f32_16x16x32_bf16 a[76:79], a[180:183], v[140:143], a[76:79]\n"
        "ds_read_b128 a[132:135], v7 offset:18432\n"
        "v_cndmask_b32_dpp v147, v155, v248, vcc row_ror:8 row_mask:0xf bank_mask:0xf\n"
        "v_mfma_f32_16x16x32_bf16 a[84:87], a[184:187], v[140:143], a[84:87]\n"
        "buffer_load_dwordx4 v180, s[36:39], s66 offen  lds\n"
        "ds_read_b128 a[136:139], v7 offset:20480\n"
        "v_mov_b32_dpp v155, v248 row_ror:8 row_mask:0xf bank_mask:0x3\n"
        "v_mfma_f32_16x16x32_bf16 a[92:95], a[188:191], v[140:143], a[92:95]\n"
        "ds_read_b128 a[140:143], v7 offset:22528\n"
        "s_waitcnt lgkmcnt(6)\n"
        "v_mfma_f32_16x16x32_bf16 a[0:3], a[96:99], v[144:147], a[0:3]\n"
        "v_add_u32 v0, -73728, v0\n"
        "v_fma_mix_f32 v225, v225, v12, v14 op_sel_hi:[1,0,0]\n"
        "v_mfma_f32_16x16x32_bf16 a[8:11], a[100:103], v[144:147], a[8:11]\n"
        "ds_read_b128 a[144:147], v0 offset:0\n"
        "s_add_u32 m0, s55, 34816\n"
        "v_mfma_f32_16x16x32_bf16 a[16:19], a[104:107], v[144:147], a[16:19]\n"
        "v_fma_mix_f32 v229, v229, v12, v14 op_sel_hi:[1,0,0]\n"
        "ds_read_b128 a[148:151], v0 offset:2048\n"
        "v_fma_mix_f32 v227, v227, v13, v15 op_sel_hi:[1,0,0]\n"
        "v_mfma_f32_16x16x32_bf16 a[24:27], a[108:111], v[144:147], a[24:27]\n"
        "ds_read_b128 a[152:155], v0 offset:4096\n"
        "ds_read_b128 a[156:159], v0 offset:6144\n"
        "v_mfma_f32_16x16x32_bf16 a[32:35], a[112:115], v[144:147], a[32:35]\n"
        "v_fma_mix_f32 v231, v231, v13, v15 op_sel_hi:[1,0,0]\n"
        "buffer_load_dwordx4 v181, s[36:39], s66 offen  lds\n"
        "v_mfma_f32_16x16x32_bf16 a[40:43], a[116:119], v[144:147], a[40:43]\n"
        "ds_read_b128 a[160:163], v0 offset:8192\n"
        "v_fma_mix_f32 v233, v233, v12, v14 op_sel_hi:[1,0,0]\n"
        "s_waitcnt lgkmcnt(5)\n"
        "v_mfma_f32_16x16x32_bf16 a[48:51], a[120:123], v[144:147], a[48:51]\n"
        "ds_read_b128 a[164:167], v0 offset:10240\n"
        "v_fma_mix_f32 v237, v237, v12, v14 op_sel_hi:[1,0,0]\n"
        "ds_read_b128 a[168:171], v0 offset:12288\n"
        "v_mfma_f32_16x16x32_bf16 a[56:59], a[124:127], v[144:147], a[56:59]\n"
        "s_add_u32 m0, s55, 36864\n"
        "v_fma_mix_f32 v235, v235, v13, v15 op_sel_hi:[1,0,0]\n"
        "v_mfma_f32_16x16x32_bf16 a[64:67], a[128:131], v[144:147], a[64:67]\n"
        "ds_read_b128 a[172:175], v0 offset:14336\n"
        "ds_read_b128 a[176:179], v0 offset:16384\n"
        "v_mfma_f32_16x16x32_bf16 a[72:75], a[132:135], v[144:147], a[72:75]\n"
        "v_fma_mix_f32 v239, v239, v13, v15 op_sel_hi:[1,0,0]\n"
        "ds_read_b128 a[180:183], v0 offset:18432\n"
        "v_mfma_f32_16x16x32_bf16 a[80:83], a[136:139], v[144:147], a[80:83]\n"
        "v_cvt_pk_bf16_f32 v225, v225, v229\n"
        "buffer_load_dwordx4 v182, s[36:39], s66 offen  lds\n"
        "ds_read_b128 a[184:187], v0 offset:20480\n"
        "v_mfma_f32_16x16x32_bf16 a[88:91], a[140:143], v[144:147], a[88:91]\n"
        "v_cvt_pk_bf16_f32 v156, v227, v231\n"
        "ds_read_b128 a[188:191], v0 offset:22528\n"
        "s_waitcnt lgkmcnt(6)\n"
        "v_mfma_f32_16x16x32_bf16 a[0:3], a[144:147], v[152:155], a[0:3]\n"
        "v_fma_mix_f32 v241, v241, v12, v14 op_sel_hi:[1,0,0]\n"
        "v_fma_mix_f32 v245, v245, v12, v14 op_sel_hi:[1,0,0]\n"
        "v_mfma_f32_16x16x32_bf16 a[8:11], a[148:151], v[152:155], a[8:11]\n"
        "v_fma_mix_f32 v243, v243, v13, v15 op_sel_hi:[1,0,0]\n"
        "s_add_u32 m0, s55, 38912\n"
        "v_mfma_f32_16x16x32_bf16 a[16:19], a[152:155], v[152:155], a[16:19]\n"
        "v_fma_mix_f32 v247, v247, v13, v15 op_sel_hi:[1,0,0]\n"
        "v_cvt_pk_bf16_f32 v233, v233, v237\n"
        "v_mfma_f32_16x16x32_bf16 a[24:27], a[156:159], v[152:155], a[24:27]\n"
        "v_cvt_pk_bf16_f32 v157, v235, v239\n"
        "v_cndmask_b32_dpp v148, v156, v225, vcc row_ror:8 row_mask:0xf bank_mask:0xf\n"
        "v_mov_b32_dpp v156, v225 row_ror:8 row_mask:0xf bank_mask:0x3\n"
        "v_mfma_f32_16x16x32_bf16 a[32:35], a[160:163], v[152:155], a[32:35]\n"
        "buffer_load_dwordx4 v183, s[36:39], s66 offen  lds\n"
        "v_fma_mix_f32 v249, v249, v12, v14 op_sel_hi:[1,0,0]\n"
        "v_mfma_f32_16x16x32_bf16 a[40:43], a[164:167], v[152:155], a[40:43]\n"
        "v_fma_mix_f32 v253, v253, v12, v14 op_sel_hi:[1,0,0]\n"
        "v_fma_mix_f32 v251, v251, v13, v15 op_sel_hi:[1,0,0]\n"
        "s_waitcnt lgkmcnt(0)\n"
        "v_mfma_f32_16x16x32_bf16 a[48:51], a[168:171], v[152:155], a[48:51]\n"
        "v_fma_mix_f32 v255, v255, v13, v15 op_sel_hi:[1,0,0]\n"
        "v_cvt_pk_bf16_f32 v241, v241, v245\n"
        "v_mfma_f32_16x16x32_bf16 a[56:59], a[172:175], v[152:155], a[56:59]\n"
        "v_cvt_pk_bf16_f32 v158, v243, v247\n"
        "s_add_u32 m0, s55, 40960\n"
        "v_mfma_f32_16x16x32_bf16 a[64:67], a[176:179], v[152:155], a[64:67]\n"
        "v_cndmask_b32_dpp v149, v157, v233, vcc row_ror:8 row_mask:0xf bank_mask:0xf\n"
        "v_mov_b32_dpp v157, v233 row_ror:8 row_mask:0xf bank_mask:0x3\n"
        "v_mfma_f32_16x16x32_bf16 a[72:75], a[180:183], v[152:155], a[72:75]\n"
        "v_cvt_pk_bf16_f32 v249, v249, v253\n"
        "v_cvt_pk_bf16_f32 v159, v251, v255\n"
        "v_cndmask_b32_dpp v150, v158, v241, vcc row_ror:8 row_mask:0xf bank_mask:0xf\n"
        "v_mfma_f32_16x16x32_bf16 a[80:83], a[184:187], v[152:155], a[80:83]\n"
        "buffer_load_dwordx4 v184, s[36:39], s66 offen  lds\n"
        "v_mov_b32_dpp v158, v241 row_ror:8 row_mask:0xf bank_mask:0x3\n"
        "v_mfma_f32_16x16x32_bf16 a[88:91], a[188:191], v[152:155], a[88:91]\n"
        "v_cndmask_b32_dpp v151, v159, v249, vcc row_ror:8 row_mask:0xf bank_mask:0xf\n"
        "v_mov_b32_dpp v159, v249 row_ror:8 row_mask:0xf bank_mask:0x3\n"
        "s_nop 0\n"
        "v_mfma_f32_16x16x32_bf16 a[4:7], a[96:99], v[148:151], a[4:7]\n"
        "v_add_u32 v208, 0, v7\n"
        "s_add_u32 m0, s55, 61440\n"
        "v_mfma_f32_16x16x32_bf16 a[12:15], a[100:103], v[148:151], a[12:15]\n"
        "v_add_u32 v211, 0, v0\n"
        "v_mfma_f32_16x16x32_bf16 a[20:23], a[104:107], v[148:151], a[20:23]\n"
        "v_add_u32 v209, 65536, v7\n"
        "v_add_u32 v212, 65536, v0\n"
        "v_mfma_f32_16x16x32_bf16 a[28:31], a[108:111], v[148:151], a[28:31]\n"
        "buffer_load_dwordx4 v176, s[36:39], s67 offen  lds\n"
        "v_add_u32 v210, 131072, v7\n"
        "v_mfma_f32_16x16x32_bf16 a[36:39], a[112:115], v[148:151], a[36:39]\n"
        "v_add_u32 v213, 131072, v0\n"
        "v_mfma_f32_16x16x32_bf16 a[44:47], a[116:119], v[148:151], a[44:47]\n"
        "s_add_u32 m0, s55, 63488\n"
        "v_mbcnt_lo_u32_b32 v214, -1, 0\n"
        "v_mfma_f32_16x16x32_bf16 a[52:55], a[120:123], v[148:151], a[52:55]\n"
        "v_mbcnt_hi_u32_b32 v214, -1, v214\n"
        "buffer_load_dwordx4 v177, s[36:39], s67 offen  lds\n"
        "v_mfma_f32_16x16x32_bf16 a[60:63], a[124:127], v[148:151], a[60:63]\n"
        "v_lshrrev_b32 v215, 3, v214\n"
        "v_mfma_f32_16x16x32_bf16 a[68:71], a[128:131], v[148:151], a[68:71]\n"
        "v_mul_u32_u24 v215, 18432, v215\n"
        "v_and_b32 v214, 7, v214\n"
        "v_mfma_f32_16x16x32_bf16 a[76:79], a[132:135], v[148:151], a[76:79]\n"
        "s_add_u32 m0, s55, 65536\n"
        "v_lshl_add_u32 v214, v214, 3, %[oc]\n"
        "v_mfma_f32_16x16x32_bf16 a[84:87], a[136:139], v[148:151], a[84:87]\n"
        "v_add_u32 v214, v214, v215\n"
        "v_mfma_f32_16x16x32_bf16 a[92:95], a[140:143], v[148:151], a[92:95]\n"
        "buffer_load_dwordx4 v178, s[36:39], s67 offen  lds\n"
        "v_add_u32 v215, 9216, v214\n"
        "s_waitcnt vmcnt(12)\n"
        "s_barrier\n"
        "v_mfma_f32_16x16x32_bf16 a[4:7], a[144:147], v[156:159], a[4:7]\n"
        "ds_read_b128 a[96:99], v209 offset:32768\n"
        "v_mfma_f32_16x16x32_bf16 a[12:15], a[148:151], v[156:159], a[12:15]\n"
        "s_add_u32 m0, s55, 67584\n"
        "v_mfma_f32_16x16x32_bf16 a[20:23], a[152:155], v[156:159], a[20:23]\n"
        "ds_read_b128 a[100:103], v212 offset:32768\n"
        "v_mfma_f32_16x16x32_bf16 a[28:31], a[156:159], v[156:159], a[28:31]\n"
        "ds_read_b128 a[104:107], v209 offset:34816\n"
        "v_mfma_f32_16x16x32_bf16 a[36:39], a[160:163], v[156:159], a[36:39]\n"
        "buffer_load_dwordx4 v179, s[36:39], s67 offen  lds\n"
        "v_mfma_f32_16x16x32_bf16 a[44:47], a[164:167], v[156:159], a[44:47]\n"
        "ds_read_b128 a[108:111], v212 offset:34816\n"
        "v_mfma_f32_16x16x32_bf16 a[52:55], a[168:171], v[156:159], a[52:55]\n"
        "s_add_u32 m0, s55, 69632\n"
        "v_mfma_f32_16x16x32_bf16 a[60:63], a[172:175], v[156:159], a[60:63]\n"
        "ds_read_b128 a[112:115], v209 offset:36864\n"
        "v_mfma_f32_16x16x32_bf16 a[68:71], a[176:179], v[156:159], a[68:71]\n"
        "ds_read_b128 a[116:119], v212 offset:36864\n"
        "v_mfma_f32_16x16x32_bf16 a[76:79], a[180:183], v[156:159], a[76:79]\n"
        "buffer_load_dwordx4 v180, s[36:39], s67 offen  lds\n"
        "v_mfma_f32_16x16x32_bf16 a[84:87], a[184:187], v[156:159], a[84:87]\n"
        "ds_read_b128 a[120:123], v209 offset:38912\n"
        "v_mfma_f32_16x16x32_bf16 a[92:95], a[188:191], v[156:159], a[92:95]\n"
        "s_waitcnt lgkmcnt(4)\n"
        "v_mfma_f32_16x16x32_bf16 v[160:163], a[96:99], v[16:19], 0\n"
        "ds_read_b128 a[124:127], v212 offset:38912\n"
        "v_mfma_f32_16x16x32_bf16 v[164:167], a[96:99], v[20:23], 0\n"
        "s_add_u32 m0, s55, 71680\n"
        "v_mfma_f32_16x16x32_bf16 v[160:163], a[100:103], v[24:27], v[160:163]\n"
        "v_accvgpr_read_b32 v192, a0\n"
        "ds_read_b128 a[128:131], v209 offset:40960\n"
        "v_accvgpr_read_b32 v196, a4\n"
        "v_mfma_f32_16x16x32_bf16 v[164:167], a[100:103], v[28:31], v[164:167]\n"
        "v_accvgpr_read_b32 v194, a2\n"
        "v_mfma_f32_16x16x32_bf16 v[160:163], a[104:107], v[32:35], v[160:163]\n"
        "v_accvgpr_read_b32 v198, a6\n"
        "ds_read_b128 a[132:135], v212 offset:40960\n"
        "buffer_load_dwordx4 v181, s[36:39], s67 offen  lds\n"
        "v_cvt_pk_bf16_f32 v192, v192, v196\n"
        "v_mfma_f32_16x16x32_bf16 v[164:167], a[104:107], v[36:39], v[164:167]\n"
        "v_accvgpr_read_b32 v193, a1\n"
        "s_waitcnt lgkmcnt(4)\n"
        "v_mfma_f32_16x16x32_bf16 v[160:163], a[108:111], v[40:43], v[160:163]\n"
        "v_accvgpr_read_b32 v197, a5\n"
        "ds_read_b128 a[136:139], v209 offset:43008\n"
        "v_cvt_pk_bf16_f32 v225, v194, v198\n"
        "v_mfma_f32_16x16x32_bf16 v[164:167], a[108:111], v[44:47], v[164:167]\n"
        "v_accvgpr_read_b32 v195, a3\n"
        "s_add_u32 m0, s55, 73728\n"
        "v_accvgpr_read_b32 v199, a7\n"
        "v_mfma_f32_16x16x32_bf16 v[160:163], a[112:115], v[48:51], v[160:163]\n"
        "ds_read_b128 a[140:143], v212 offset:43008\n"
        "v_cvt_pk_bf16_f32 v193, v193, v197\n"
        "v_mfma_f32_16x16x32_bf16 v[164:167], a[112:115], v[52:55], v[164:167]\n"
        "v_cvt_pk_bf16_f32 v227, v195, v199\n"
        "v_cndmask_b32_dpp v224, v225, v192, vcc row_ror:8 row_mask:0xf bank_mask:0xf\n"
        "v_mfma_f32_16x16x32_bf16 v[160:163], a[116:119], v[56:59], v[160:163]\n"
        "ds_read_b128 a[144:147], v209 offset:45056\n"
        "v_mov_b32_dpp v225, v192 row_ror:8 row_mask:0xf bank_mask:0x3\n"
        "buffer_load_dwordx4 v182, s[36:39], s67 offen  lds\n"
        "v_mfma_f32_16x16x32_bf16 v[164:167], a[116:119], v[60:63], v[164:167]\n"
        "v_cndmask_b32_dpp v226, v227, v193, vcc row_ror:8 row_mask:0xf bank_mask:0xf\n"
        "buffer_store_dwordx2 v[224:225], v214, s[40:43], s69 offen sc0 sc1 nt\n"
        "s_waitcnt lgkmcnt(4)\n"
        "v_mfma_f32_16x16x32_bf16 v[160:163], a[120:123], v[64:67], v[160:163]\n"
        "v_mov_b32_dpp v227, v193 row_ror:8 row_mask:0xf bank_mask:0x3\n"
        "ds_read_b128 a[148:151], v212 offset:45056\n"
        "v_mfma_f32_16x16x32_bf16 v[164:167], a[120:123], v[68:71], v[164:167]\n"
        "buffer_store_dwordx2 v[226:227], v215, s[40:43], s69 offen sc0 sc1 nt\n"
        "s_add_u32 m0, s55, 75776\n"
        "v_accvgpr_read_b32 v200, a8\n"
        "v_mfma_f32_16x16x32_bf16 v[160:163], a[124:127], v[72:75], v[160:163]\n"
        "v_accvgpr_read_b32 v204, a12\n"
        "ds_read_b128 a[152:155], v209 offset:47104\n"
        "v_accvgpr_read_b32 v202, a10\n"
        "v_mfma_f32_16x16x32_bf16 v[164:167], a[124:127], v[76:79], v[164:167]\n"
        "v_accvgpr_read_b32 v206, a14\n"
        "v_mfma_f32_16x16x32_bf16 v[160:163], a[128:131], v[80:83], v[160:163]\n"
        "v_cvt_pk_bf16_f32 v200, v200, v204\n"
        "ds_read_b128 a[156:159], v212 offset:47104\n"
        "buffer_load_dwordx4 v183, s[36:39], s67 offen  lds\n"
        "v_accvgpr_read_b32 v201, a9\n"
        "v_mfma_f32_16x16x32_bf16 v[164:167], a[128:131], v[84:87], v[164:167]\n"
        "v_accvgpr_read_b32 v205, a13\n"
        "s_waitcnt lgkmcnt(4)\n"
        "v_mfma_f32_16x16x32_bf16 v[160:163], a[132:135], v[88:91], v[160:163]\n"
        "v_cvt_pk_bf16_f32 v229, v202, v206\n"
        "ds_read_b128 a[160:163], v209 offset:49152\n"
        "v_accvgpr_read_b32 v203, a11\n"
        "v_mfma_f32_16x16x32_bf16 v[164:167], a[132:135], v[92:95], v[164:167]\n"
        "v_accvgpr_read_b32 v207, a15\n"
        "s_add_u32 m0, s55, 77824\n"
        "v_cvt_pk_bf16_f32 v201, v201, v205\n"
        "v_mfma_f32_16x16x32_bf16 v[160:163], a[136:139], v[96:99], v[160:163]\n"
        "ds_read_b128 a[164:167], v212 offset:49152\n"
        "v_cvt_pk_bf16_f32 v231, v203, v207\n"
        "v_mfma_f32_16x16x32_bf16 v[164:167], a[136:139], v[100:103], v[164:167]\n"
        "v_cndmask_b32_dpp v228, v229, v200, vcc row_ror:8 row_mask:0xf bank_mask:0xf\n"
        "v_mov_b32_dpp v229, v200 row_ror:8 row_mask:0xf bank_mask:0x3\n"
        "v_mfma_f32_16x16x32_bf16 v[160:163], a[140:143], v[104:107], v[160:163]\n"
        "v_cndmask_b32_dpp v230, v231, v201, vcc row_ror:8 row_mask:0xf bank_mask:0xf\n"
        "ds_read_b128 a[168:171], v209 offset:51200\n"
        "buffer_load_dwordx4 v184, s[36:39], s67 offen  lds\n"
        "v_mfma_f32_16x16x32_bf16 v[164:167], a[140:143], v[108:111], v[164:167]\n"
        "buffer_store_dwordx2 v[228:229], v214, s[40:43], s70 offen sc0 sc1 nt\n"
        "v_mov_b32_dpp v231, v201 row_ror:8 row_mask:0xf bank_mask:0x3\n"
        "s_waitcnt lgkmcnt(4)\n"
        "v_mfma_f32_16x16x32_bf16 v[160:163], a[144:147], v[112:115], v[160:163]\n"
        "buffer_store_dwordx2 v[230:231], v215, s[40:43], s70 offen sc0 sc1 nt\n"
        "ds_read_b128 a[172:175], v212 offset:51200\n"
        "v_accvgpr_read_b32 v192, a16\n"
        "v_mfma_f32_16x16x32_bf16 v[164:167], a[144:147], v[116:119], v[164:167]\n"
        "v_accvgpr_read_b32 v196, a20\n"
        "v_mfma_f32_16x16x32_bf16 v[160:163], a[148:151], v[120:123], v[160:163]\n"
        "v_accvgpr_read_b32 v194, a18\n"
        "ds_read_b128 a[176:179], v209 offset:53248\n"
        "v_accvgpr_read_b32 v198, a22\n"
        "v_mfma_f32_16x16x32_bf16 v[164:167], a[148:151], v[124:127], v[164:167]\n"
        "v_cvt_pk_bf16_f32 v192, v192, v196\n"
        "v_mfma_f32_16x16x32_bf16 v[160:163], a[152:155], v[128:131], v[160:163]\n"
        "v_accvgpr_read_b32 v193, a17\n"
        "ds_read_b128 a[180:183], v212 offset:53248\n"
        "v_accvgpr_read_b32 v197, a21\n"
        "v_mfma_f32_16x16x32_bf16 v[164:167], a[152:155], v[132:135], v[164:167]\n"
        "v_cvt_pk_bf16_f32 v233, v194, v198\n"
        "s_waitcnt lgkmcnt(4)\n"
        "v_mfma_f32_16x16x32_bf16 v[160:163], a[156:159], v[136:139], v[160:163]\n"
        "v_accvgpr_read_b32 v195, a19\n"
        "ds_read_b128 a[184:187], v209 offset:55296\n"
        "v_accvgpr_read_b32 v199, a23\n"
        "v_mfma_f32_16x16x32_bf16 v[164:167], a[156:159], v[140:143], v[164:167]\n"
        "v_cvt_pk_bf16_f32 v193, v193, v197\n"
        "v_cvt_pk_bf16_f32 v235, v195, v199\n"
        "v_mfma_f32_16x16x32_bf16 v[160:163], a[160:163], v[144:147], v[160:163]\n"
        "ds_read_b128 a[188:191], v212 offset:55296\n"
        "v_cndmask_b32_dpp v232, v233, v192, vcc row_ror:8 row_mask:0xf bank_mask:0xf\n"
        "v_mfma_f32_16x16x32_bf16 v[164:167], a[160:163], v[148:151], v[164:167]\n"
        "v_mov_b32_dpp v233, v192 row_ror:8 row_mask:0xf bank_mask:0x3\n"
        "v_cndmask_b32_dpp v234, v235, v193, vcc row_ror:8 row_mask:0xf bank_mask:0xf\n"
        "v_mfma_f32_16x16x32_bf16 v[160:163], a[164:167], v[152:155], v[160:163]\n"
        "buffer_store_dwordx2 v[232:233], v214, s[40:43], s71 offen sc0 sc1 nt\n"
        "ds_read_b128 a[96:99], v209 offset:57344\n"
        "v_mfma_f32_16x16x32_bf16 v[164:167], a[164:167], v[156:159], v[164:167]\n"
        "v_mov_b32_dpp v235, v193 row_ror:8 row_mask:0xf bank_mask:0x3\n"
        "buffer_store_dwordx2 v[234:235], v215, s[40:43], s71 offen sc0 sc1 nt\n"
        "s_waitcnt lgkmcnt(4)\n"
        "v_mfma_f32_16x16x32_bf16 v[168:171], a[168:171], v[16:19], 0\n"
        "ds_read_b128 a[100:103], v212 offset:57344\n"
        "s_add_u32 m0, s98, 0\n"
        "v_mfma_f32_16x16x32_bf16 v[172:175], a[168:171], v[20:23], 0\n"
        "v_mfma_f32_16x16x32_bf16 v[168:171], a[172:175], v[24:27], v[168:171]\n"
        "buffer_load_dwordx4 v176, s[36:39], s68 offen  lds\n"
        "v_cvt_pk_bf16_f32 v160, v160, v164\n"
        "ds_read_b128 a[104:107], v209 offset:59392\n"
        "v_cvt_pk_bf16_f32 v237, v162, v166\n"
        "v_mfma_f32_16x16x32_bf16 v[172:175], a[172:175], v[28:31], v[172:175]\n"
        "v_cvt_pk_bf16_f32 v161, v161, v165\n"
        "s_add_u32 m0, s98, 2048\n"
        "v_cvt_pk_bf16_f32 v239, v163, v167\n"
        "v_mfma_f32_16x16x32_bf16 v[168:171], a[176:179], v[32:35], v[168:171]\n"
        "v_cndmask_b32_dpp v236, v237, v160, vcc row_ror:8 row_mask:0xf bank_mask:0xf\n"
        "ds_read_b128 a[108:111], v212 offset:59392\n"
        "buffer_load_dwordx4 v177, s[36:39], s68 offen  lds\n"
        "v_mov_b32_dpp v237, v160 row_ror:8 row_mask:0xf bank_mask:0x3\n"
        "v_mfma_f32_16x16x32_bf16 v[172:175], a[176:179], v[36:39], v[172:175]\n"
        "v_cndmask_b32_dpp v238, v239, v161, vcc row_ror:8 row_mask:0xf bank_mask:0xf\n"
        "buffer_store_dwordx2 v[236:237], v214, s[40:43], s81 offen sc0 sc1 nt\n"
        "s_waitcnt lgkmcnt(4)\n"
        "v_mfma_f32_16x16x32_bf16 v[168:171], a[180:183], v[40:43], v[168:171]\n"
        "s_add_u32 m0, s98, 4096\n"
        "ds_read_b128 a[112:115], v209 offset:61440\n"
        "v_mov_b32_dpp v239, v161 row_ror:8 row_mask:0xf bank_mask:0x3\n"
        "v_mfma_f32_16x16x32_bf16 v[172:175], a[180:183], v[44:47], v[172:175]\n"
        "buffer_store_dwordx2 v[238:239], v215, s[40:43], s81 offen sc0 sc1 nt\n"
        "buffer_load_dwordx4 v178, s[36:39], s68 offen  lds\n"
        "v_accvgpr_read_b32 v200, a24\n"
        "v_mfma_f32_16x16x32_bf16 v[168:171], a[184:187], v[48:51], v[168:171]\n"
        "v_accvgpr_read_b32 v204, a28\n"
        "ds_read_b128 a[116:119], v212 offset:61440\n"
        "v_accvgpr_read_b32 v202, a26\n"
        "s_add_u32 m0, s98, 6144\n"
        "v_mfma_f32_16x16x32_bf16 v[172:175], a[184:187], v[52:55], v[172:175]\n"
        "v_accvgpr_read_b32 v206, a30\n"
        "v_cvt_pk_bf16_f32 v200, v200, v204\n"
        "v_mfma_f32_16x16x32_bf16 v[168:171], a[188:191], v[56:59], v[168:171]\n"
        "buffer_load_dwordx4 v179, s[36:39], s68 offen  lds\n"
        "v_accvgpr_read_b32 v201, a25\n"
        "ds_read_b128 a[120:123], v209 offset:63488\n"
        "v_accvgpr_read_b32 v205, a29\n"
        "v_mfma_f32_16x16x32_bf16 v[172:175], a[188:191], v[60:63], v[172:175]\n"
        "v_cvt_pk_bf16_f32 v225, v202, v206\n"
        "s_add_u32 m0, s98, 8192\n"
        "v_accvgpr_read_b32 v203, a27\n"
        "s_waitcnt lgkmcnt(4)\n"
        "v_mfma_f32_16x16x32_bf16 v[168:171], a[96:99], v[64:67], v[168:171]\n"
        "v_accvgpr_read_b32 v207, a31\n"
        "ds_read_b128 a[124:127], v212 offset:63488\n"
        "buffer_load_dwordx4 v180, s[36:39], s68 offen  lds\n"
        "v_cvt_pk_bf16_f32 v201, v201, v205\n"
        "v_mfma_f32_16x16x32_bf16 v[172:175], a[96:99], v[68:71], v[172:175]\n"
        "v_cvt_pk_bf16_f32 v227, v203, v207\n"
        "v_cndmask_b32_dpp v224, v225, v200, vcc row_ror:8 row_mask:0xf bank_mask:0xf\n"
        "v_mfma_f32_16x16x32_bf16 v[168:171], a[100:103], v[72:75], v[168:171]\n"
        "s_add_u32 m0, s98, 10240\n"
        "v_mov_b32_dpp v225, v200 row_ror:8 row_mask:0xf bank_mask:0x3\n"
        "ds_read_b128 a[128:131], v210 offset:0\n"
        "v_mfma_f32_16x16x32_bf16 v[172:175], a[100:103], v[76:79], v[172:175]\n"
        "v_cndmask_b32_dpp v226, v227, v201, vcc row_ror:8 row_mask:0xf bank_mask:0xf\n"
        "buffer_load_dwordx4 v181, s[36:39], s68 offen  lds\n"
        "buffer_store_dwordx2 v[224:225], v214, s[40:43], s72 offen sc0 sc1 nt\n"
        "v_mfma_f32_16x16x32_bf16 v[168:171], a[104:107], v[80:83], v[168:171]\n"
        "v_mov_b32_dpp v227, v201 row_ror:8 row_mask:0xf bank_mask:0x3\n"
        "ds_read_b128 a[132:135], v213 offset:0\n"
        "buffer_store_dwordx2 v[226:227], v215, s[40:43], s72 offen sc0 sc1 nt\n"
        "s_add_u32 m0, s98, 12288\n"
        "v_mfma_f32_16x16x32_bf16 v[172:175], a[104:107], v[84:87], v[172:175]\n"
        "v_accvgpr_read_b32 v192, a32\n"
        "v_accvgpr_read_b32 v196, a36\n"
        "s_waitcnt lgkmcnt(4)\n"
        "v_mfma_f32_16x16x32_bf16 v[168:171], a[108:111], v[88:91], v[168:171]\n"
        "buffer_load_dwordx4 v182, s[36:39], s68 offen  lds\n"
        "v_accvgpr_read_b32 v194, a34\n"
        "ds_read_b128 a[136:139], v210 offset:2048\n"
        "v_accvgpr_read_b32 v198, a38\n"
        "v_mfma_f32_16x16x32_bf16 v[172:175], a[108:111], v[92:95], v[172:175]\n"
        "v_cvt_pk_bf16_f32 v192, v192, v196\n"
        "s_add_u32 m0, s98, 14336\n"
        "v_accvgpr_read_b32 v193, a33\n"
        "v_mfma_f32_16x16x32_bf16 v[168:171], a[112:115], v[96:99], v[168:171]\n"
        "v_accvgpr_read_b32 v197, a37\n"
        "ds_read_b128 a[140:143], v213 offset:2048\n"
        "buffer_load_dwordx4 v183, s[36:39], s68 offen  lds\n"
        "v_cvt_pk_bf16_f32 v229, v194, v198\n"
        "v_mfma_f32_16x16x32_bf16 v[172:175], a[112:115], v[100:103], v[172:175]\n"
        "v_accvgpr_read_b32 v195, a35\n"
        "v_accvgpr_read_b32 v199, a39\n"
        "v_mfma_f32_16x16x32_bf16 v[168:171], a[116:119], v[104:107], v[168:171]\n"
        "s_add_u32 m0, s98, 16384\n"
        "s_waitcnt vmcnt(27)\n"
        "s_barrier\n"
        "v_cvt_pk_bf16_f32 v193, v193, v197\n"
        "ds_read_b128 a[144:147], v208 offset:24576\n"
        "v_cvt_pk_bf16_f32 v231, v195, v199\n"
        "v_mfma_f32_16x16x32_bf16 v[172:175], a[116:119], v[108:111], v[172:175]\n"
        "buffer_load_dwordx4 v184, s[36:39], s68 offen  lds\n"
        "v_cndmask_b32_dpp v228, v229, v192, vcc row_ror:8 row_mask:0xf bank_mask:0xf\n"
        "s_waitcnt lgkmcnt(4)\n"
        "v_mfma_f32_16x16x32_bf16 v[168:171], a[120:123], v[112:115], v[168:171]\n"
        "v_mov_b32_dpp v229, v192 row_ror:8 row_mask:0xf bank_mask:0x3\n"
        "ds_read_b128 a[148:151], v211 offset:24576\n"
        "v_cndmask_b32_dpp v230, v231, v193, vcc row_ror:8 row_mask:0xf bank_mask:0xf\n"
        "v_mfma_f32_16x16x32_bf16 v[172:175], a[120:123], v[116:119], v[172:175]\n"
        "buffer_store_dwordx2 v[228:229], v214, s[40:43], s73 offen sc0 sc1 nt\n"
        "v_mov_b32_dpp v231, v193 row_ror:8 row_mask:0xf bank_mask:0x3\n"
        "v_mfma_f32_16x16x32_bf16 v[168:171], a[124:127], v[120:123], v[168:171]\n"
        "buffer_store_dwordx2 v[230:231], v215, s[40:43], s73 offen sc0 sc1 nt\n"
        "ds_read_b128 a[152:155], v208 offset:26624\n"
        "v_accvgpr_read_b32 v200, a40\n"
        "v_mfma_f32_16x16x32_bf16 v[172:175], a[124:127], v[124:127], v[172:175]\n"
        "v_accvgpr_read_b32 v204, a44\n"
        "v_accvgpr_read_b32 v202, a42\n"
        "v_mfma_f32_16x16x32_bf16 v[168:171], a[128:131], v[128:131], v[168:171]\n"
        "v_accvgpr_read_b32 v206, a46\n"
        "ds_read_b128 a[156:159], v211 offset:26624\n"
        "v_cvt_pk_bf16_f32 v200, v200, v204\n"
        "v_mfma_f32_16x16x32_bf16 v[172:175], a[128:131], v[132:135], v[172:175]\n"
        "v_accvgpr_read_b32 v201, a41\n"
        "v_accvgpr_read_b32 v205, a45\n"
        "s_waitcnt lgkmcnt(4)\n"
        "v_mfma_f32_16x16x32_bf16 v[168:171], a[132:135], v[136:139], v[168:171]\n"
        "v_cvt_pk_bf16_f32 v233, v202, v206\n"
        "ds_read_b128 a[160:163], v208 offset:28672\n"
        "v_accvgpr_read_b32 v203, a43\n"
        "v_mfma_f32_16x16x32_bf16 v[172:175], a[132:135], v[140:143], v[172:175]\n"
        "v_accvgpr_read_b32 v207, a47\n"
        "v_mfma_f32_16x16x32_bf16 v[168:171], a[136:139], v[144:147], v[168:171]\n"
        "v_cvt_pk_bf16_f32 v201, v201, v205\n"
        "ds_read_b128 a[164:167], v211 offset:28672\n"
        "v_cvt_pk_bf16_f32 v235, v203, v207\n"
        "v_mfma_f32_16x16x32_bf16 v[172:175], a[136:139], v[148:151], v[172:175]\n"
        "v_cndmask_b32_dpp v232, v233, v200, vcc row_ror:8 row_mask:0xf bank_mask:0xf\n"
        "v_mov_b32_dpp v233, v200 row_ror:8 row_mask:0xf bank_mask:0x3\n"
        "v_mfma_f32_16x16x32_bf16 v[168:171], a[140:143], v[152:155], v[168:171]\n"
        "v_cndmask_b32_dpp v234, v235, v201, vcc row_ror:8 row_mask:0xf bank_mask:0xf\n"
        "ds_read_b128 a[168:171], v208 offset:30720\n"
        "buffer_store_dwordx2 v[232:233], v214, s[40:43], s74 offen sc0 sc1 nt\n"
        "v_mfma_f32_16x16x32_bf16 v[172:175], a[140:143], v[156:159], v[172:175]\n"
        "v_mov_b32_dpp v235, v201 row_ror:8 row_mask:0xf bank_mask:0x3\n"
        "buffer_store_dwordx2 v[234:235], v215, s[40:43], s74 offen sc0 sc1 nt\n"
        "s_waitcnt lgkmcnt(4)\n"
        "v_mfma_f32_16x16x32_bf16 v[160:163], a[144:147], v[16:19], 0\n"
        "ds_read_b128 a[172:175], v211 offset:30720\n"
        "v_mfma_f32_16x16x32_bf16 v[164:167], a[144:147], v[20:23], 0\n"
        "v_mfma_f32_16x16x32_bf16 v[160:163], a[148:151], v[24:27], v[160:163]\n"
        "s_nop 0\n"
        "v_cvt_pk_bf16_f32 v168, v168, v172\n"
        "ds_read_b128 a[176:179], v208 offset:32768\n"
        "v_cvt_pk_bf16_f32 v237, v170, v174\n"
        "v_mfma_f32_16x16x32_bf16 v[164:167], a[148:151], v[28:31], v[164:167]\n"
        "v_cvt_pk_bf16_f32 v169, v169, v173\n"
        "v_cvt_pk_bf16_f32 v239, v171, v175\n"
        "v_mfma_f32_16x16x32_bf16 v[160:163], a[152:155], v[32:35], v[160:163]\n"
        "v_cndmask_b32_dpp v236, v237, v168, vcc row_ror:8 row_mask:0xf bank_mask:0xf\n"
        "ds_read_b128 a[180:183], v211 offset:32768\n"
        "v_mov_b32_dpp v237, v168 row_ror:8 row_mask:0xf bank_mask:0x3\n"
        "v_mfma_f32_16x16x32_bf16 v[164:167], a[152:155], v[36:39], v[164:167]\n"
        "v_cndmask_b32_dpp v238, v239, v169, vcc row_ror:8 row_mask:0xf bank_mask:0xf\n"
        "buffer_store_dwordx2 v[236:237], v214, s[40:43], s82 offen sc0 sc1 nt\n"
        "s_waitcnt lgkmcnt(4)\n"
        "v_mfma_f32_16x16x32_bf16 v[160:163], a[156:159], v[40:43], v[160:163]\n"
        "ds_read_b128 a[184:187], v208 offset:34816\n"
        "v_mov_b32_dpp v239, v169 row_ror:8 row_mask:0xf bank_mask:0x3\n"
        "v_mfma_f32_16x16x32_bf16 v[164:167], a[156:159], v[44:47], v[164:167]\n"
        "buffer_store_dwordx2 v[238:239], v215, s[40:43], s82 offen sc0 sc1 nt\n"
        "v_accvgpr_read_b32 v192, a48\n"
        "v_mfma_f32_16x16x32_bf16 v[160:163], a[160:163], v[48:51], v[160:163]\n"
        "v_accvgpr_read_b32 v196, a52\n"
        "ds_read_b128 a[188:191], v211 offset:34816\n"
        "v_accvgpr_read_b32 v194, a50\n"
        "v_mfma_f32_16x16x32_bf16 v[164:167], a[160:163], v[52:55], v[164:167]\n"
        "v_accvgpr_read_b32 v198, a54\n"
        "v_cvt_pk_bf16_f32 v192, v192, v196\n"
        "v_mfma_f32_16x16x32_bf16 v[160:163], a[164:167], v[56:59], v[160:163]\n"
        "v_accvgpr_read_b32 v193, a49\n"
        "ds_read_b128 a[96:99], v208 offset:36864\n"
        "v_accvgpr_read_b32 v197, a53\n"
        "v_mfma_f32_16x16x32_bf16 v[164:167], a[164:167], v[60:63], v[164:167]\n"
        "v_cvt_pk_bf16_f32 v225, v194, v198\n"
        "v_accvgpr_read_b32 v195, a51\n"
        "s_waitcnt lgkmcnt(4)\n"
        "v_mfma_f32_16x16x32_bf16 v[160:163], a[168:171], v[64:67], v[160:163]\n"
        "v_accvgpr_read_b32 v199, a55\n"
        "ds_read_b128 a[100:103], v211 offset:36864\n"
        "v_cvt_pk_bf16_f32 v193, v193, v197\n"
        "v_mfma_f32_16x16x32_bf16 v[164:167], a[168:171], v[68:71], v[164:167]\n"
        "v_cvt_pk_bf16_f32 v227, v195, v199\n"
        "v_cndmask_b32_dpp v224, v225, v192, vcc row_ror:8 row_mask:0xf bank_mask:0xf\n"
        "v_mfma_f32_16x16x32_bf16 v[160:163], a[172:175], v[72:75], v[160:163]\n"
        "v_mov_b32_dpp v225, v192 row_ror:8 row_mask:0xf bank_mask:0x3\n"
        "ds_read_b128 a[104:107], v208 offset:38912\n"
        "v_mfma_f32_16x16x32_bf16 v[164:167], a[172:175], v[76:79], v[164:167]\n"
        "v_cndmask_b32_dpp v226, v227, v193, vcc row_ror:8 row_mask:0xf bank_mask:0xf\n"
        "buffer_store_dwordx2 v[224:225], v214, s[40:43], s75 offen sc0 sc1 nt\n"
        "v_mfma_f32_16x16x32_bf16 v[160:163], a[176:179], v[80:83], v[160:163]\n"
        "v_mov_b32_dpp v227, v193 row_ror:8 row_mask:0xf bank_mask:0x3\n"
        "ds_read_b128 a[108:111], v211 offset:38912\n"
        "buffer_store_dwordx2 v[226:227], v215, s[40:43], s75 offen sc0 sc1 nt\n"
        "v_mfma_f32_16x16x32_bf16 v[164:167], a[176:179], v[84:87], v[164:167]\n"
        "v_accvgpr_read_b32 v200, a56\n"
        "v_accvgpr_read_b32 v204, a60\n"
        "s_waitcnt lgkmcnt(4)\n"
        "v_mfma_f32_16x16x32_bf16 v[160:163], a[180:183], v[88:91], v[160:163]\n"
        "v_accvgpr_read_b32 v202, a58\n"
        "ds_read_b128 a[112:115], v208 offset:40960\n"
        "v_accvgpr_read_b32 v206, a62\n"
        "v_mfma_f32_16x16x32_bf16 v[164:167], a[180:183], v[92:95], v[164:167]\n"
        "v_cvt_pk_bf16_f32 v200, v200, v204\n"
        "v_accvgpr_read_b32 v201, a57\n"
        "v_mfma_f32_16x16x32_bf16 v[160:163], a[184:187], v[96:99], v[160:163]\n"
        "v_accvgpr_read_b32 v205, a61\n"
        "ds_read_b128 a[116:119], v211 offset:40960\n"
        "v_cvt_pk_bf16_f32 v229, v202, v206\n"
        "v_mfma_f32_16x16x32_bf16 v[164:167], a[184:187], v[100:103], v[164:167]\n"
        "v_accvgpr_read_b32 v203, a59\n"
        "v_accvgpr_read_b32 v207, a63\n"
        "v_mfma_f32_16x16x32_bf16 v[160:163], a[188:191], v[104:107], v[160:163]\n"
        "v_cvt_pk_bf16_f32 v201, v201, v205\n"
        "ds_read_b128 a[120:123], v208 offset:43008\n"
        "v_cvt_pk_bf16_f32 v231, v203, v207\n"
        "v_mfma_f32_16x16x32_bf16 v[164:167], a[188:191], v[108:111], v[164:167]\n"
        "v_cndmask_b32_dpp v228, v229, v200, vcc row_ror:8 row_mask:0xf bank_mask:0xf\n"
        "s_waitcnt lgkmcnt(4)\n"
        "v_mfma_f32_16x16x32_bf16 v[160:163], a[96:99], v[112:115], v[160:163]\n"
        "v_mov_b32_dpp v229, v200 row_ror:8 row_mask:0xf bank_mask:0x3\n"
        "ds_read_b128 a[124:127], v211 offset:43008\n"
        "v_cndmask_b32_dpp v230, v231, v201, vcc row_ror:8 row_mask:0xf bank_mask:0xf\n"
        "v_mfma_f32_16x16x32_bf16 v[164:167], a[96:99], v[116:119], v[164:167]\n"
        "buffer_store_dwordx2 v[228:229], v214, s[40:43], s76 offen sc0 sc1 nt\n"
        "v_mov_b32_dpp v231, v201 row_ror:8 row_mask:0xf bank_mask:0x3\n"
        "v_mfma_f32_16x16x32_bf16 v[160:163], a[100:103], v[120:123], v[160:163]\n"
        "buffer_store_dwordx2 v[230:231], v215, s[40:43], s76 offen sc0 sc1 nt\n"
        "ds_read_b128 a[128:131], v208 offset:45056\n"
        "v_accvgpr_read_b32 v192, a64\n"
        "v_mfma_f32_16x16x32_bf16 v[164:167], a[100:103], v[124:127], v[164:167]\n"
        "v_accvgpr_read_b32 v196, a68\n"
        "v_accvgpr_read_b32 v194, a66\n"
        "v_mfma_f32_16x16x32_bf16 v[160:163], a[104:107], v[128:131], v[160:163]\n"
        "v_accvgpr_read_b32 v198, a70\n"
        "ds_read_b128 a[132:135], v211 offset:45056\n"
        "v_cvt_pk_bf16_f32 v192, v192, v196\n"
        "v_mfma_f32_16x16x32_bf16 v[164:167], a[104:107], v[132:135], v[164:167]\n"
        "v_accvgpr_read_b32 v193, a65\n"
        "v_accvgpr_read_b32 v197, a69\n"
        "s_waitcnt lgkmcnt(4)\n"
        "v_mfma_f32_16x16x32_bf16 v[160:163], a[108:111], v[136:139], v[160:163]\n"
        "v_cvt_pk_bf16_f32 v233, v194, v198\n"
        "ds_read_b128 a[136:139], v208 offset:47104\n"
        "v_accvgpr_read_b32 v195, a67\n"
        "v_mfma_f32_16x16x32_bf16 v[164:167], a[108:111], v[140:143], v[164:167]\n"
        "v_accvgpr_read_b32 v199, a71\n"
        "v_mfma_f32_16x16x32_bf16 v[160:163], a[112:115], v[144:147], v[160:163]\n"
        "v_cvt_pk_bf16_f32 v193, v193, v197\n"
        "ds_read_b128 a[140:143], v211 offset:47104\n"
        "v_cvt_pk_bf16_f32 v235, v195, v199\n"
        "v_mfma_f32_16x16x32_bf16 v[164:167], a[112:115], v[148:151], v[164:167]\n"
        "v_cndmask_b32_dpp v232, v233, v192, vcc row_ror:8 row_mask:0xf bank_mask:0xf\n"
        "v_mov_b32_dpp v233, v192 row_ror:8 row_mask:0xf bank_mask:0x3\n"
        "v_mfma_f32_16x16x32_bf16 v[160:163], a[116:119], v[152:155], v[160:163]\n"
        "v_cndmask_b32_dpp v234, v235, v193, vcc row_ror:8 row_mask:0xf bank_mask:0xf\n"
        "ds_read_b128 a[144:147], v208 offset:49152\n"
        "buffer_store_dwordx2 v[232:233], v214, s[40:43], s77 offen sc0 sc1 nt\n"
        "v_mfma_f32_16x16x32_bf16 v[164:167], a[116:119], v[156:159], v[164:167]\n"
        "v_mov_b32_dpp v235, v193 row_ror:8 row_mask:0xf bank_mask:0x3\n"
        "buffer_store_dwordx2 v[234:235], v215, s[40:43], s77 offen sc0 sc1 nt\n"
        "s_waitcnt lgkmcnt(4)\n"
        "v_mfma_f32_16x16x32_bf16 v[168:171], a[120:123], v[16:19], 0\n"
        "ds_read_b128 a[148:151], v211 offset:49152\n"
        "v_mfma_f32_16x16x32_bf16 v[172:175], a[120:123], v[20:23], 0\n"
        "v_mfma_f32_16x16x32_bf16 v[168:171], a[124:127], v[24:27], v[168:171]\n"
        "s_nop 0\n"
        "v_cvt_pk_bf16_f32 v160, v160, v164\n"
        "ds_read_b128 a[152:155], v208 offset:51200\n"
        "v_cvt_pk_bf16_f32 v237, v162, v166\n"
        "v_mfma_f32_16x16x32_bf16 v[172:175], a[124:127], v[28:31], v[172:175]\n"
        "v_cvt_pk_bf16_f32 v161, v161, v165\n"
        "v_cvt_pk_bf16_f32 v239, v163, v167\n"
        "v_mfma_f32_16x16x32_bf16 v[168:171], a[128:131], v[32:35], v[168:171]\n"
        "v_cndmask_b32_dpp v236, v237, v160, vcc row_ror:8 row_mask:0xf bank_mask:0xf\n"
        "ds_read_b128 a[156:159], v211 offset:51200\n"
        "v_mov_b32_dpp v237, v160 row_ror:8 row_mask:0xf bank_mask:0x3\n"
        "v_mfma_f32_16x16x32_bf16 v[172:175], a[128:131], v[36:39], v[172:175]\n"
        "v_cndmask_b32_dpp v238, v239, v161, vcc row_ror:8 row_mask:0xf bank_mask:0xf\n"
        "buffer_store_dwordx2 v[236:237], v214, s[40:43], s83 offen sc0 sc1 nt\n"
        "s_waitcnt lgkmcnt(4)\n"
        "v_mfma_f32_16x16x32_bf16 v[168:171], a[132:135], v[40:43], v[168:171]\n"
        "ds_read_b128 a[160:163], v208 offset:53248\n"
        "v_mov_b32_dpp v239, v161 row_ror:8 row_mask:0xf bank_mask:0x3\n"
        "v_mfma_f32_16x16x32_bf16 v[172:175], a[132:135], v[44:47], v[172:175]\n"
        "buffer_store_dwordx2 v[238:239], v215, s[40:43], s83 offen sc0 sc1 nt\n"
        "v_accvgpr_read_b32 v200, a72\n"
        "v_mfma_f32_16x16x32_bf16 v[168:171], a[136:139], v[48:51], v[168:171]\n"
        "v_accvgpr_read_b32 v204, a76\n"
        "ds_read_b128 a[164:167], v211 offset:53248\n"
        "v_accvgpr_read_b32 v202, a74\n"
        "v_mfma_f32_16x16x32_bf16 v[172:175], a[136:139], v[52:55], v[172:175]\n"
        "v_accvgpr_read_b32 v206, a78\n"
        "v_cvt_pk_bf16_f32 v200, v200, v204\n"
        "v_mfma_f32_16x16x32_bf16 v[168:171], a[140:143], v[56:59], v[168:171]\n"
        "v_accvgpr_read_b32 v201, a73\n"
        "ds_read_b128 a[168:171], v208 offset:55296\n"
        "v_accvgpr_read_b32 v205, a77\n"
        "v_mfma_f32_16x16x32_bf16 v[172:175], a[140:143], v[60:63], v[172:175]\n"
        "v_cvt_pk_bf16_f32 v225, v202, v206\n"
        "v_accvgpr_read_b32 v203, a75\n"
        "s_waitcnt lgkmcnt(4)\n"
        "v_mfma_f32_16x16x32_bf16 v[168:171], a[144:147], v[64:67], v[168:171]\n"
        "v_accvgpr_read_b32 v207, a79\n"
        "ds_read_b128 a[172:175], v211 offset:55296\n"
        "v_cvt_pk_bf16_f32 v201, v201, v205\n"
        "v_mfma_f32_16x16x32_bf16 v[172:175], a[144:147], v[68:71], v[172:175]\n"
        "v_cvt_pk_bf16_f32 v227, v203, v207\n"
        "v_cndmask_b32_dpp v224, v225, v200, vcc row_ror:8 row_mask:0xf bank_mask:0xf\n"
        "v_mfma_f32_16x16x32_bf16 v[168:171], a[148:151], v[72:75], v[168:171]\n"
        "v_mov_b32_dpp v225, v200 row_ror:8 row_mask:0xf bank_mask:0x3\n"
        "ds_read_b128 a[176:179], v208 offset:57344\n"
        "v_mfma_f32_16x16x32_bf16 v[172:175], a[148:151], v[76:79], v[172:175]\n"
        "v_cndmask_b32_dpp v226, v227, v201, vcc row_ror:8 row_mask:0xf bank_mask:0xf\n"
        "buffer_store_dwordx2 v[224:225], v214, s[40:43], s78 offen sc0 sc1 nt\n"
        "v_mfma_f32_16x16x32_bf16 v[168:171], a[152:155], v[80:83], v[168:171]\n"
        "v_mov_b32_dpp v227, v201 row_ror:8 row_mask:0xf bank_mask:0x3\n"
        "ds_read_b128 a[180:183], v211 offset:57344\n"
        "buffer_store_dwordx2 v[226:227], v215, s[40:43], s78 offen sc0 sc1 nt\n"
        "v_mfma_f32_16x16x32_bf16 v[172:175], a[152:155], v[84:87], v[172:175]\n"
        "v_accvgpr_read_b32 v192, a80\n"
        "v_accvgpr_read_b32 v196, a84\n"
        "s_waitcnt lgkmcnt(4)\n"
        "v_mfma_f32_16x16x32_bf16 v[168:171], a[156:159], v[88:91], v[168:171]\n"
        "v_accvgpr_read_b32 v194, a82\n"
        "ds_read_b128 a[184:187], v208 offset:59392\n"
        "v_accvgpr_read_b32 v198, a86\n"
        "v_mfma_f32_16x16x32_bf16 v[172:175], a[156:159], v[92:95], v[172:175]\n"
        "v_cvt_pk_bf16_f32 v192, v192, v196\n"
        "v_accvgpr_read_b32 v193, a81\n"
        "v_mfma_f32_16x16x32_bf16 v[168:171], a[160:163], v[96:99], v[168:171]\n"
        "v_accvgpr_read_b32 v197, a85\n"
        "ds_read_b128 a[188:191], v211 offset:59392\n"
        "v_cvt_pk_bf16_f32 v229, v194, v198\n"
        "v_mfma_f32_16x16x32_bf16 v[172:175], a[160:163], v[100:103], v[172:175]\n"
        "v_accvgpr_read_b32 v195, a83\n"
        "v_accvgpr_read_b32 v199, a87\n"
        "v_mfma_f32_16x16x32_bf16 v[168:171], a[164:167], v[104:107], v[168:171]\n"
        "s_waitcnt vmcnt(33)\n"
        "s_barrier\n"
        "v_cvt_pk_bf16_f32 v193, v193, v197\n"
        "ds_read_b128 a[96:99], v208 offset:61440\n"
        "v_cvt_pk_bf16_f32 v231, v195, v199\n"
        "v_mfma_f32_16x16x32_bf16 v[172:175], a[164:167], v[108:111], v[172:175]\n"
        "v_cndmask_b32_dpp v228, v229, v192, vcc row_ror:8 row_mask:0xf bank_mask:0xf\n"
        "s_waitcnt lgkmcnt(4)\n"
        "v_mfma_f32_16x16x32_bf16 v[168:171], a[168:171], v[112:115], v[168:171]\n"
        "v_mov_b32_dpp v229, v192 row_ror:8 row_mask:0xf bank_mask:0x3\n"
        "ds_read_b128 a[100:103], v211 offset:61440\n"
        "v_cndmask_b32_dpp v230, v231, v193, vcc row_ror:8 row_mask:0xf bank_mask:0xf\n"
        "v_mfma_f32_16x16x32_bf16 v[172:175], a[168:171], v[116:119], v[172:175]\n"
        "buffer_store_dwordx2 v[228:229], v214, s[40:43], s79 offen sc0 sc1 nt\n"
        "v_mov_b32_dpp v231, v193 row_ror:8 row_mask:0xf bank_mask:0x3\n"
        "v_mfma_f32_16x16x32_bf16 v[168:171], a[172:175], v[120:123], v[168:171]\n"
        "buffer_store_dwordx2 v[230:231], v215, s[40:43], s79 offen sc0 sc1 nt\n"
        "ds_read_b128 a[104:107], v208 offset:63488\n"
        "v_accvgpr_read_b32 v200, a88\n"
        "v_mfma_f32_16x16x32_bf16 v[172:175], a[172:175], v[124:127], v[172:175]\n"
        "v_accvgpr_read_b32 v204, a92\n"
        "v_accvgpr_read_b32 v202, a90\n"
        "v_mfma_f32_16x16x32_bf16 v[168:171], a[176:179], v[128:131], v[168:171]\n"
        "v_accvgpr_read_b32 v206, a94\n"
        "ds_read_b128 a[108:111], v211 offset:63488\n"
        "v_cvt_pk_bf16_f32 v200, v200, v204\n"
        "v_mfma_f32_16x16x32_bf16 v[172:175], a[176:179], v[132:135], v[172:175]\n"
        "v_accvgpr_read_b32 v201, a89\n"
        "v_accvgpr_read_b32 v205, a93\n"
        "s_waitcnt lgkmcnt(4)\n"
        "v_mfma_f32_16x16x32_bf16 v[168:171], a[180:183], v[136:139], v[168:171]\n"
        "v_cvt_pk_bf16_f32 v233, v202, v206\n"
        "ds_read_b128 a[112:115], v209 offset:0\n"
        "v_accvgpr_read_b32 v203, a91\n"
        "v_mfma_f32_16x16x32_bf16 v[172:175], a[180:183], v[140:143], v[172:175]\n"
        "v_accvgpr_read_b32 v207, a95\n"
        "v_mfma_f32_16x16x32_bf16 v[168:171], a[184:187], v[144:147], v[168:171]\n"
        "v_cvt_pk_bf16_f32 v201, v201, v205\n"
        "ds_read_b128 a[116:119], v212 offset:0\n"
        "v_cvt_pk_bf16_f32 v235, v203, v207\n"
        "v_mfma_f32_16x16x32_bf16 v[172:175], a[184:187], v[148:151], v[172:175]\n"
        "v_cndmask_b32_dpp v232, v233, v200, vcc row_ror:8 row_mask:0xf bank_mask:0xf\n"
        "v_mov_b32_dpp v233, v200 row_ror:8 row_mask:0xf bank_mask:0x3\n"
        "v_mfma_f32_16x16x32_bf16 v[168:171], a[188:191], v[152:155], v[168:171]\n"
        "v_cndmask_b32_dpp v234, v235, v201, vcc row_ror:8 row_mask:0xf bank_mask:0xf\n"
        "ds_read_b128 a[120:123], v209 offset:2048\n"
        "buffer_store_dwordx2 v[232:233], v214, s[40:43], s80 offen sc0 sc1 nt\n"
        "v_mfma_f32_16x16x32_bf16 v[172:175], a[188:191], v[156:159], v[172:175]\n"
        "v_mov_b32_dpp v235, v201 row_ror:8 row_mask:0xf bank_mask:0x3\n"
        "buffer_store_dwordx2 v[234:235], v215, s[40:43], s80 offen sc0 sc1 nt\n"
        "s_waitcnt lgkmcnt(4)\n"
        "v_mfma_f32_16x16x32_bf16 v[160:163], a[96:99], v[16:19], 0\n"
        "ds_read_b128 a[124:127], v212 offset:2048\n"
        "v_mfma_f32_16x16x32_bf16 v[164:167], a[96:99], v[20:23], 0\n"
        "v_mfma_f32_16x16x32_bf16 v[160:163], a[100:103], v[24:27], v[160:163]\n"
        "ds_read_b128 a[128:131], v209 offset:4096\n"
        "v_cvt_pk_bf16_f32 v168, v168, v172\n"
        "v_mfma_f32_16x16x32_bf16 v[164:167], a[100:103], v[28:31], v[164:167]\n"
        "v_cvt_pk_bf16_f32 v237, v170, v174\n"
        "v_mfma_f32_16x16x32_bf16 v[160:163], a[104:107], v[32:35], v[160:163]\n"
        "ds_read_b128 a[132:135], v212 offset:4096\n"
        "v_cvt_pk_bf16_f32 v169, v169, v173\n"
        "v_mfma_f32_16x16x32_bf16 v[164:167], a[104:107], v[36:39], v[164:167]\n"
        "v_cvt_pk_bf16_f32 v239, v171, v175\n"
        "s_waitcnt lgkmcnt(4)\n"
        "v_mfma_f32_16x16x32_bf16 v[160:163], a[108:111], v[40:43], v[160:163]\n"
        "ds_read_b128 a[136:139], v209 offset:6144\n"
        "v_cndmask_b32_dpp v236, v237, v168, vcc row_ror:8 row_mask:0xf bank_mask:0xf\n"
        "v_mfma_f32_16x16x32_bf16 v[164:167], a[108:111], v[44:47], v[164:167]\n"
        "v_mov_b32_dpp v237, v168 row_ror:8 row_mask:0xf bank_mask:0x3\n"
        "v_mfma_f32_16x16x32_bf16 v[160:163], a[112:115], v[48:51], v[160:163]\n"
        "ds_read_b128 a[140:143], v212 offset:6144\n"
        "v_cndmask_b32_dpp v238, v239, v169, vcc row_ror:8 row_mask:0xf bank_mask:0xf\n"
        "v_mfma_f32_16x16x32_bf16 v[164:167], a[112:115], v[52:55], v[164:167]\n"
        "buffer_store_dwordx2 v[236:237], v214, s[40:43], s84 offen sc0 sc1 nt\n"
        "v_mfma_f32_16x16x32_bf16 v[160:163], a[116:119], v[56:59], v[160:163]\n"
        "ds_read_b128 a[144:147], v209 offset:8192\n"
        "v_mov_b32_dpp v239, v169 row_ror:8 row_mask:0xf bank_mask:0x3\n"
        "v_mfma_f32_16x16x32_bf16 v[164:167], a[116:119], v[60:63], v[164:167]\n"
        "buffer_store_dwordx2 v[238:239], v215, s[40:43], s84 offen sc0 sc1 nt\n"
        "s_waitcnt lgkmcnt(4)\n"
        "v_mfma_f32_16x16x32_bf16 v[160:163], a[120:123], v[64:67], v[160:163]\n"
        "ds_read_b128 a[148:151], v212 offset:8192\n"
        "v_mfma_f32_16x16x32_bf16 v[164:167], a[120:123], v[68:71], v[164:167]\n"
        "v_mfma_f32_16x16x32_bf16 v[160:163], a[124:127], v[72:75], v[160:163]\n"
        "ds_read_b128 a[152:155], v209 offset:10240\n"
        "v_mfma_f32_16x16x32_bf16 v[164:167], a[124:127], v[76:79], v[164:167]\n"
        "v_mfma_f32_16x16x32_bf16 v[160:163], a[128:131], v[80:83], v[160:163]\n"
        "ds_read_b128 a[156:159], v212 offset:10240\n"
        "v_mfma_f32_16x16x32_bf16 v[164:167], a[128:131], v[84:87], v[164:167]\n"
        "s_waitcnt lgkmcnt(4)\n"
        "v_mfma_f32_16x16x32_bf16 v[160:163], a[132:135], v[88:91], v[160:163]\n"
        "ds_read_b128 a[160:163], v209 offset:12288\n"
        "v_mfma_f32_16x16x32_bf16 v[164:167], a[132:135], v[92:95], v[164:167]\n"
        "v_mfma_f32_16x16x32_bf16 v[160:163], a[136:139], v[96:99], v[160:163]\n"
        "ds_read_b128 a[164:167], v212 offset:12288\n"
        "v_mfma_f32_16x16x32_bf16 v[164:167], a[136:139], v[100:103], v[164:167]\n"
        "v_mfma_f32_16x16x32_bf16 v[160:163], a[140:143], v[104:107], v[160:163]\n"
        "ds_read_b128 a[168:171], v209 offset:14336\n"
        "v_mfma_f32_16x16x32_bf16 v[164:167], a[140:143], v[108:111], v[164:167]\n"
        "s_waitcnt lgkmcnt(4)\n"
        "v_mfma_f32_16x16x32_bf16 v[160:163], a[144:147], v[112:115], v[160:163]\n"
        "ds_read_b128 a[172:175], v212 offset:14336\n"
        "v_mfma_f32_16x16x32_bf16 v[164:167], a[144:147], v[116:119], v[164:167]\n"
        "v_mfma_f32_16x16x32_bf16 v[160:163], a[148:151], v[120:123], v[160:163]\n"
        "ds_read_b128 a[176:179], v209 offset:16384\n"
        "v_mfma_f32_16x16x32_bf16 v[164:167], a[148:151], v[124:127], v[164:167]\n"
        "v_mfma_f32_16x16x32_bf16 v[160:163], a[152:155], v[128:131], v[160:163]\n"
        "ds_read_b128 a[180:183], v212 offset:16384\n"
        "v_mfma_f32_16x16x32_bf16 v[164:167], a[152:155], v[132:135], v[164:167]\n"
        "s_waitcnt lgkmcnt(4)\n"
        "v_mfma_f32_16x16x32_bf16 v[160:163], a[156:159], v[136:139], v[160:163]\n"
        "ds_read_b128 a[184:187], v209 offset:18432\n"
        "v_mfma_f32_16x16x32_bf16 v[164:167], a[156:159], v[140:143], v[164:167]\n"
        "v_mfma_f32_16x16x32_bf16 v[160:163], a[160:163], v[144:147], v[160:163]\n"
        "ds_read_b128 a[188:191], v212 offset:18432\n"
        "v_mfma_f32_16x16x32_bf16 v[164:167], a[160:163], v[148:151], v[164:167]\n"
        "v_mfma_f32_16x16x32_bf16 v[160:163], a[164:167], v[152:155], v[160:163]\n"
        "ds_read_b128 a[96:99], v209 offset:20480\n"
        "v_mfma_f32_16x16x32_bf16 v[164:167], a[164:167], v[156:159], v[164:167]\n"
        "s_waitcnt lgkmcnt(4)\n"
        "v_mfma_f32_16x16x32_bf16 v[168:171], a[168:171], v[16:19], 0\n"
        "ds_read_b128 a[100:103], v212 offset:20480\n"
        "v_mfma_f32_16x16x32_bf16 v[172:175], a[168:171], v[20:23], 0\n"
        "v_mfma_f32_16x16x32_bf16 v[168:171], a[172:175], v[24:27], v[168:171]\n"
        "ds_read_b128 a[104:107], v209 offset:22528\n"
        "s_nop 1\n"
        "v_cvt_pk_bf16_f32 v160, v160, v164\n"
        "v_mfma_f32_16x16x32_bf16 v[172:175], a[172:175], v[28:31], v[172:175]\n"
        "v_cvt_pk_bf16_f32 v225, v162, v166\n"
        "v_mfma_f32_16x16x32_bf16 v[168:171], a[176:179], v[32:35], v[168:171]\n"
        "ds_read_b128 a[108:111], v212 offset:22528\n"
        "v_cvt_pk_bf16_f32 v161, v161, v165\n"
        "v_mfma_f32_16x16x32_bf16 v[172:175], a[176:179], v[36:39], v[172:175]\n"
        "v_cvt_pk_bf16_f32 v227, v163, v167\n"
        "s_waitcnt lgkmcnt(4)\n"
        "v_mfma_f32_16x16x32_bf16 v[168:171], a[180:183], v[40:43], v[168:171]\n"
        "ds_read_b128 a[112:115], v209 offset:24576\n"
        "v_cndmask_b32_dpp v224, v225, v160, vcc row_ror:8 row_mask:0xf bank_mask:0xf\n"
        "v_mfma_f32_16x16x32_bf16 v[172:175], a[180:183], v[44:47], v[172:175]\n"
        "v_mov_b32_dpp v225, v160 row_ror:8 row_mask:0xf bank_mask:0x3\n"
        "v_mfma_f32_16x16x32_bf16 v[168:171], a[184:187], v[48:51], v[168:171]\n"
        "ds_read_b128 a[116:119], v212 offset:24576\n"
        "v_cndmask_b32_dpp v226, v227, v161, vcc row_ror:8 row_mask:0xf bank_mask:0xf\n"
        "v_mfma_f32_16x16x32_bf16 v[172:175], a[184:187], v[52:55], v[172:175]\n"
        "buffer_store_dwordx2 v[224:225], v214, s[40:43], s85 offen sc0 sc1 nt\n"
        "v_mfma_f32_16x16x32_bf16 v[168:171], a[188:191], v[56:59], v[168:171]\n"
        "ds_read_b128 a[120:123], v209 offset:26624\n"
        "v_mov_b32_dpp v227, v161 row_ror:8 row_mask:0xf bank_mask:0x3\n"
        "v_mfma_f32_16x16x32_bf16 v[172:175], a[188:191], v[60:63], v[172:175]\n"
        "buffer_store_dwordx2 v[226:227], v215, s[40:43], s85 offen sc0 sc1 nt\n"
        "s_waitcnt lgkmcnt(4)\n"
        "v_mfma_f32_16x16x32_bf16 v[168:171], a[96:99], v[64:67], v[168:171]\n"
        "ds_read_b128 a[124:127], v212 offset:26624\n"
        "v_mfma_f32_16x16x32_bf16 v[172:175], a[96:99], v[68:71], v[172:175]\n"
        "v_mfma_f32_16x16x32_bf16 v[168:171], a[100:103], v[72:75], v[168:171]\n"
        "ds_read_b128 a[128:131], v209 offset:28672\n"
        "v_mfma_f32_16x16x32_bf16 v[172:175], a[100:103], v[76:79], v[172:175]\n"
        "v_mfma_f32_16x16x32_bf16 v[168:171], a[104:107], v[80:83], v[168:171]\n"
        "ds_read_b128 a[132:135], v212 offset:28672\n"
        "v_mfma_f32_16x16x32_bf16 v[172:175], a[104:107], v[84:87], v[172:175]\n"
        "s_waitcnt lgkmcnt(4)\n"
        "v_mfma_f32_16x16x32_bf16 v[168:171], a[108:111], v[88:91], v[168:171]\n"
        "ds_read_b128 a[136:139], v209 offset:30720\n"
        "v_mfma_f32_16x16x32_bf16 v[172:175], a[108:111], v[92:95], v[172:175]\n"
        "v_mfma_f32_16x16x32_bf16 v[168:171], a[112:115], v[96:99], v[168:171]\n"
        "ds_read_b128 a[140:143], v212 offset:30720\n"
        "v_mfma_f32_16x16x32_bf16 v[172:175], a[112:115], v[100:103], v[172:175]\n"
        "v_mfma_f32_16x16x32_bf16 v[168:171], a[116:119], v[104:107], v[168:171]\n"
        "s_waitcnt vmcnt(24)\n"
        "s_barrier\n"
        "ds_read_b128 a[144:147], v208 offset:0\n"
        "v_mfma_f32_16x16x32_bf16 v[172:175], a[116:119], v[108:111], v[172:175]\n"
        "s_waitcnt lgkmcnt(4)\n"
        "v_mfma_f32_16x16x32_bf16 v[168:171], a[120:123], v[112:115], v[168:171]\n"
        "ds_read_b128 a[148:151], v211 offset:0\n"
        "v_mfma_f32_16x16x32_bf16 v[172:175], a[120:123], v[116:119], v[172:175]\n"
        "v_mfma_f32_16x16x32_bf16 v[168:171], a[124:127], v[120:123], v[168:171]\n"
        "ds_read_b128 a[152:155], v208 offset:2048\n"
        "v_mfma_f32_16x16x32_bf16 v[172:175], a[124:127], v[124:127], v[172:175]\n"
        "v_mfma_f32_16x16x32_bf16 v[168:171], a[128:131], v[128:131], v[168:171]\n"
        "ds_read_b128 a[156:159], v211 offset:2048\n"
        "v_mfma_f32_16x16x32_bf16 v[172:175], a[128:131], v[132:135], v[172:175]\n"
        "s_waitcnt lgkmcnt(4)\n"
        "v_mfma_f32_16x16x32_bf16 v[168:171], a[132:135], v[136:139], v[168:171]\n"
        "ds_read_b128 a[160:163], v208 offset:4096\n"
        "v_mfma_f32_16x16x32_bf16 v[172:175], a[132:135], v[140:143], v[172:175]\n"
        "v_mfma_f32_16x16x32_bf16 v[168:171], a[136:139], v[144:147], v[168:171]\n"
        "ds_read_b128 a[164:167], v211 offset:4096\n"
        "v_mfma_f32_16x16x32_bf16 v[172:175], a[136:139], v[148:151], v[172:175]\n"
        "v_mfma_f32_16x16x32_bf16 v[168:171], a[140:143], v[152:155], v[168:171]\n"
        "ds_read_b128 a[168:171], v208 offset:6144\n"
        "v_mfma_f32_16x16x32_bf16 v[172:175], a[140:143], v[156:159], v[172:175]\n"
        "s_cmp_eq_u32 %[nt], 18\n"
        "s_cbranch_scc1 .Lt18_%=\n"
        "s_waitcnt lgkmcnt(4)\n"
        "v_mfma_f32_16x16x32_bf16 v[160:163], a[144:147], v[16:19], 0\n"
        "ds_read_b128 a[172:175], v211 offset:6144\n"
        "v_mfma_f32_16x16x32_bf16 v[164:167], a[144:147], v[20:23], 0\n"
        "v_mfma_f32_16x16x32_bf16 v[160:163], a[148:151], v[24:27], v[160:163]\n"
        "ds_read_b128 a[176:179], v208 offset:8192\n"
        "v_cvt_pk_bf16_f32 v168, v168, v172\n"
        "v_mfma_f32_16x16x32_bf16 v[164:167], a[148:151], v[28:31], v[164:167]\n"
        "v_cvt_pk_bf16_f32 v229, v170, v174\n"
        "v_mfma_f32_16x16x32_bf16 v[160:163], a[152:155], v[32:35], v[160:163]\n"
        "ds_read_b128 a[180:183], v211 offset:8192\n"
        "v_cvt_pk_bf16_f32 v169, v169, v173\n"
        "v_mfma_f32_16x16x32_bf16 v[164:167], a[152:155], v[36:39], v[164:167]\n"
        "v_cvt_pk_bf16_f32 v231, v171, v175\n"
        "s_waitcnt lgkmcnt(4)\n"
        "v_mfma_f32_16x16x32_bf16 v[160:163], a[156:159], v[40:43], v[160:163]\n"
        "ds_read_b128 a[184:187], v208 offset:10240\n"
        "v_cndmask_b32_dpp v228, v229, v168, vcc row_ror:8 row_mask:0xf bank_mask:0xf\n"
        "v_mfma_f32_16x16x32_bf16 v[164:167], a[156:159], v[44:47], v[164:167]\n"
        "v_mov_b32_dpp v229, v168 row_ror:8 row_mask:0xf bank_mask:0x3\n"
        "v_mfma_f32_16x16x32_bf16 v[160:163], a[160:163], v[48:51], v[160:163]\n"
        "ds_read_b128 a[188:191], v211 offset:10240\n"
        "v_cndmask_b32_dpp v230, v231, v169, vcc row_ror:8 row_mask:0xf bank_mask:0xf\n"
        "v_mfma_f32_16x16x32_bf16 v[164:167], a[160:163], v[52:55], v[164:167]\n"
        "buffer_store_dwordx2 v[228:229], v214, s[40:43], s86 offen sc0 sc1 nt\n"
        "v_mfma_f32_16x16x32_bf16 v[160:163], a[164:167], v[56:59], v[160:163]\n"
        "ds_read_b128 a[96:99], v208 offset:12288\n"
        "v_mov_b32_dpp v231, v169 row_ror:8 row_mask:0xf bank_mask:0x3\n"
        "v_mfma_f32_16x16x32_bf16 v[164:167], a[164:167], v[60:63], v[164:167]\n"
        "buffer_store_dwordx2 v[230:231], v215, s[40:43], s86 offen sc0 sc1 nt\n"
        "s_waitcnt lgkmcnt(4)\n"
        "v_mfma_f32_16x16x32_bf16 v[160:163], a[168:171], v[64:67], v[160:163]\n"
        "ds_read_b128 a[100:103], v211 offset:12288\n"
        "v_mfma_f32_16x16x32_bf16 v[164:167], a[168:171], v[68:71], v[164:167]\n"
        "v_mfma_f32_16x16x32_bf16 v[160:163], a[172:175], v[72:75], v[160:163]\n"
        "ds_read_b128 a[104:107], v208 offset:14336\n"
        "v_mfma_f32_16x16x32_bf16 v[164:167], a[172:175], v[76:79], v[164:167]\n"
        "v_mfma_f32_16x16x32_bf16 v[160:163], a[176:179], v[80:83], v[160:163]\n"
        "ds_read_b128 a[108:111], v211 offset:14336\n"
        "v_mfma_f32_16x16x32_bf16 v[164:167], a[176:179], v[84:87], v[164:167]\n"
        "s_waitcnt lgkmcnt(4)\n"
        "v_mfma_f32_16x16x32_bf16 v[160:163], a[180:183], v[88:91], v[160:163]\n"
        "ds_read_b128 a[112:115], v208 offset:16384\n"
        "v_mfma_f32_16x16x32_bf16 v[164:167], a[180:183], v[92:95], v[164:167]\n"
        "v_mfma_f32_16x16x32_bf16 v[160:163], a[184:187], v[96:99], v[160:163]\n"
        "ds_read_b128 a[116:119], v211 offset:16384\n"
        "v_mfma_f32_16x16x32_bf16 v[164:167], a[184:187], v[100:103], v[164:167]\n"
        "v_mfma_f32_16x16x32_bf16 v[160:163], a[188:191], v[104:107], v[160:163]\n"
        "v_mfma_f32_16x16x32_bf16 v[164:167], a[188:191], v[108:111], v[164:167]\n"
        "s_waitcnt lgkmcnt(3)\n"
        "v_mfma_f32_16x16x32_bf16 v[160:163], a[96:99], v[112:115], v[160:163]\n"
        "v_mfma_f32_16x16x32_bf16 v[164:167], a[96:99], v[116:119], v[164:167]\n"
        "v_mfma_f32_16x16x32_bf16 v[160:163], a[100:103], v[120:123], v[160:163]\n"
        "v_mfma_f32_16x16x32_bf16 v[164:167], a[100:103], v[124:127], v[164:167]\n"
        "v_mfma_f32_16x16x32_bf16 v[160:163], a[104:107], v[128:131], v[160:163]\n"
        "v_mfma_f32_16x16x32_bf16 v[164:167], a[104:107], v[132:135], v[164:167]\n"
        "s_waitcnt lgkmcnt(0)\n"
        "v_mfma_f32_16x16x32_bf16 v[160:163], a[108:111], v[136:139], v[160:163]\n"
        "v_mfma_f32_16x16x32_bf16 v[164:167], a[108:111], v[140:143], v[164:167]\n"
        "v_mfma_f32_16x16x32_bf16 v[160:163], a[112:115], v[144:147], v[160:163]\n"
        "v_mfma_f32_16x16x32_bf16 v[164:167], a[112:115], v[148:151], v[164:167]\n"
        "v_mfma_f32_16x16x32_bf16 v[160:163], a[116:119], v[152:155], v[160:163]\n"
        "v_mfma_f32_16x16x32_bf16 v[164:167], a[116:119], v[156:159], v[164:167]\n"
        "s_nop 7\n"
        "v_cvt_pk_bf16_f32 v160, v160, v164\n"
        "v_cvt_pk_bf16_f32 v233, v162, v166\n"
        "v_cvt_pk_bf16_f32 v161, v161, v165\n"
        "v_cvt_pk_bf16_f32 v235, v163, v167\n"
        "v_cndmask_b32_dpp v232, v233, v160, vcc row_ror:8 row_mask:0xf bank_mask:0xf\n"
        "v_mov_b32_dpp v233, v160 row_ror:8 row_mask:0xf bank_mask:0x3\n"
        "v_cndmask_b32_dpp v234, v235, v161, vcc row_ror:8 row_mask:0xf bank_mask:0xf\n"
        "buffer_store_dwordx2 v[232:233], v214, s[40:43], s87 offen sc0 sc1 nt\n"
        "v_mov_b32_dpp v235, v161 row_ror:8 row_mask:0xf bank_mask:0x3\n"
        "buffer_store_dwordx2 v[234:235], v215, s[40:43], s87 offen sc0 sc1 nt\n"
        "s_branch .Lend_%=\n"
        ".Lt18_%=:\n"
        "s_nop 4\n"
        "v_cvt_pk_bf16_f32 v168, v168, v172\n"
        "v_cvt_pk_bf16_f32 v237, v170, v174\n"
        "v_cvt_pk_bf16_f32 v169, v169, v173\n"
        "v_cvt_pk_bf16_f32 v239, v171, v175\n"
        "v_cndmask_b32_dpp v236, v237, v168, vcc row_ror:8 row_mask:0xf bank_mask:0xf\n"
        "v_mov_b32_dpp v237, v168 row_ror:8 row_mask:0xf bank_mask:0x3\n"
        "v_cndmask_b32_dpp v238, v239, v169, vcc row_ror:8 row_mask:0xf bank_mask:0xf\n"
        "buffer_store_dwordx2 v[236:237], v214, s[40:43], s86 offen sc0 sc1 nt\n"
        "v_mov_b32_dpp v239, v169 row_ror:8 row_mask:0xf bank_mask:0x3\n"
        "buffer_store_dwordx2 v[238:239], v215, s[40:43], s86 offen sc0 sc1 nt\n"
        ".Lend_%=:\n"
        : [tid] "+v"(tid)
        : [px] "s"(x), [pq] "s"(qw), [ps] "s"(sc), [pz] "s"(zr), [po] "s"(out), [wc] "s"(wc),
          [x0] "s"(krot * 128), [krot] "s"(krot), [ta] "s"(ta), [tk] "s"(tk), [lim] "s"(lim), [span] "s"(span), [w] "s"(w),
          [sc0] "s"(sc0), [oc] "s"(oc), [nt] "s"(nt), [lds] "s"((uint32_t)(uintptr_t)lds)
        : "memory", "vcc", "scc", "v1", "v2", "v3", "v4", "v5", "v6", "v7", "v8", "v9", "v10", "v11", "v12", "v13", "v14", "v15", "v16", "v17", "v18", "v19", "v20", "v21", "v22", "v23", "v24", "v25", "v26", "v27", "v28", "v29", "v30", "v31", "v32", "v33", "v34", "v35", "v36", "v37", "v38", "v39", "v40", "v41", "v42", "v43", "v44", "v45", "v46", "v47", "v48", "v49", "v50", "v51", "v52", "v53", "v54", "v55", "v56", "v57", "v58", "v59", "v60", "v61", "v62", "v63", "v64", "v65", "v66", "v67", "v68", "v69", "v70", "v71", "v72", "v73", "v74", "v75", "v76", "v77", "v78", "v79", "v80", "v81", "v82", "v83", "v84", "v85", "v86", "v87", "v88", "v89", "v90", "v91", "v92", "v93", "v94", "v95", "v96", "v97", "v98", "v99", "v100", "v101", "v102", "v103", "v104", "v105", "v106", "v107", "v108", "v109", "v110", "v111", "v112", "v113", "v114", "v115", "v116", "v117", "v118", "v119", "v120", "v121", "v122", "v123", "v124", "v125", "v126", "v127", "v128", "v129", "v130", "v131", "v132", "v133", "v134", "v135", "v136", "v137", "v138", "v139", "v140", "v141", "v142", "v143", "v144", "v145", "v146", "v147", "v148", "v149", "v150", "v151", "v152", "v153", "v154", "v155", "v156", "v157", "v158", "v159", "v160", "v161", "v162", "v163", "v164", "v165", "v166", "v167", "v168", "v169", "v170", "v171", "v172", "v173", "v174", "v175", "v176", "v177", "v178", "v179", "v180", "v181", "v182", "v183", "v184", "v185", "v186", "v187", "v188", "v189", "v190", "v191", "v192", "v193", "v194", "v195", "v196", "v197", "v198", "v199", "v200", "v201", "v202", "v203", "v204", "v205", "v206", "v207", "v208", "v209", "v210", "v211", "v212", "v213", "v214", "v215", "v216", "v217", "v218", "v219", "v220", "v221", "v222", "v223", "v224", "v225", "v226", "v227", "v228", "v229", "v230", "v231", "v232", "v233", "v234", "v235", "v236", "v237", "v238", "v239", "v240", "v241", "v242", "v243", "v244", "v245", "v246", "v247", "v248", "v249", "v250", "v251", "v252", "v253", "v254", "v255", "a0", "a1", "a2", "a3", "a4", "a5", "a6", "a7", "a8", "a9", "a10", "a11", "a12", "a13", "a14", "a15", "a16", "a17", "a18", "a19", "a20", "a21", "a22", "a23", "a24", "a25", "a26", "a27", "a28", "a29", "a30", "a31", "a32", "a33", "a34", "a35", "a36", "a37", "a38", "a39", "a40", "a41", "a42", "a43", "a44", "a45", "a46", "a47", "a48", "a49", "a50", "a51", "a52", "a53", "a54", "a55", "a56", "a57", "a58", "a59", "a60", "a61", "a62", "a63", "a64", "a65", "a66", "a67", "a68", "a69", "a70", "a71", "a72", "a73", "a74", "a75", "a76", "a77", "a78", "a79", "a80", "a81", "a82", "a83", "a84", "a85", "a86", "a87", "a88", "a89", "a90", "a91", "a92", "a93", "a94", "a95", "a96", "a97", "a98", "a99", "a100", "a101", "a102", "a103", "a104", "a105", "a106", "a107", "a108", "a109", "a110", "a111", "a112", "a113", "a114", "a115", "a116", "a117", "a118", "a119", "a120", "a121", "a122", "a123", "a124", "a125", "a126", "a127", "a128", "a129", "a130", "a131", "a132", "a133", "a134", "a135", "a136", "a137", "a138", "a139", "a140", "a141", "a142", "a143", "a144", "a145", "a146", "a147", "a148", "a149", "a150", "a151", "a152", "a153", "a154", "a155", "a156", "a157", "a158", "a159", "a160", "a161", "a162", "a163", "a164", "a165", "a166", "a167", "a168", "a169", "a170", "a171", "a172", "a173", "a174", "a175", "a176", "a177", "a178", "a179", "a180", "a181", "a182", "a183", "a184", "a185", "a186", "a187", "a188", "a189", "a190", "a191", "a192", "a193", "a194", "a195", "a196", "a197", "a198", "a199", "a200", "a201", "a202", "a203", "a204", "a205", "a206", "a207", "a208", "a209", "a210", "a211", "a212", "a213", "a214", "a215", "a216", "a217", "a218", "a219", "a220", "a221", "a222", "a223", "a224", "a225", "a226", "a227", "a228", "a229", "a230", "a231", "a232", "a233", "a234", "a235", "a236", "a237", "a238", "a239", "a240", "a241", "a242", "a243", "a244", "a245", "a246", "a247", "a248", "a249", "a250", "a251", "a252", "a253", "a254", "a255", "s28", "s29", "s30", "s31", "s33", "s34", "s35", "s36", "s37", "s38", "s39", "s40", "s41", "s42", "s43", "s44", "s45", "s46", "s47", "s48", "s49", "s50", "s51", "s52", "s53", "s54", "s55", "s56", "s57", "s58", "s59", "s60", "s61", "s62", "s63", "s64", "s65", "s66", "s67", "s68", "s69", "s70", "s71", "s72", "s73", "s74", "s75", "s76", "s77", "s78", "s79", "s80", "s81", "s82", "s83", "s84", "s85", "s86", "s87", "s88", "s89", "s90", "s91", "s92", "s93", "s94", "s95", "s96", "s97", "s98", "s99");
}

extern "C" void kernel_launch(void* const* d_in, const int* in_sizes, int n_in, void* d_out, int out_size,
                              void* d_ws, size_t ws_size, hipStream_t stream) {
    w4a16_gemm<<<252, 256, 0, stream>>>((const uint16_t*)d_in[0], (const int*)d_in[1], (const uint16_t*)d_in[2],
                                        (const uint16_t*)d_in[3], (uint16_t*)d_out);
}
```
